```python
import torch

def _softmax(x, axis=-1):
    return torch.softmax(x, dim=axis)
def _gelu(x):
    return torch.nn.functional.gelu(x, approximate="tanh")
def _silu(x):
    return torch.nn.functional.silu(x)
def _sigmoid(x):
    return torch.sigmoid(x)
def _layernorm(x, axis=-1, eps=1e-5):
    m = x.mean(dim=axis, keepdim=True)
    v = x.var(dim=axis, keepdim=True, unbiased=False)
    return (x - m) / torch.sqrt(v + eps)
def _scatter_add(tgt, idx, src, axis=0):
    return tgt.clone().index_add_(axis, idx, src)
def _segment_sum(src, seg, n):
    out = torch.zeros((n,) + src.shape[1:], dtype=src.dtype)
    return out.index_add_(0, seg, src)
def _pad(x, pw):
    flat = tuple(v for pair in reversed(pw) for v in pair)
    return torch.nn.functional.pad(x, flat)

kernel_name = 'standalone__gqa_block_cached__near_ridge'

def setup_inputs():

    _w = (lambda a: (lambda t: t.to(torch.bfloat16) if t.is_floating_point() else t)(torch.as_tensor(a)))
    B, Sq, D = 16, 48, 512
    Hq, Hkv, Sk, Dh = 32, 8, 512, 128
    assert Hq % Hkv == 0, (Hq, Hkv)
    return {
        "x": _w((lambda *s: torch.randn(*s, dtype=torch.bfloat16) * 0.8)(B, Sq, D)),
        "k_cache": _w((lambda *s: torch.randn(*s, dtype=torch.bfloat16) * 0.8)(B, Hkv, Sk, Dh)),
        "v_cache": _w((lambda *s: torch.randn(*s, dtype=torch.bfloat16) * 0.05)(B, Hkv, Sk, Dh)),
        "wq": _w((lambda *s: torch.randn(*s, dtype=torch.bfloat16) * 0.8)(D, Hq * Dh) * (D ** -0.5)),
        "wo": _w((lambda *s: torch.randn(*s, dtype=torch.bfloat16) * 0.05)(Hq * Dh, D) * ((Hq * Dh) ** -0.5)),
    }

def reference(x, k_cache, v_cache, wq, wo):
    B, Sq, D = x.shape
    Hq, Dh = 32, 128
    Hkv = k_cache.shape[1]
    rep = Hq // Hkv
    q = (lambda x, *d: x.permute(*d))((x @ wq).reshape(B, Sq, Hq, Dh), 0, 2, 1, 3)
    k = (lambda x, n, axis: torch.repeat_interleave(x, n, dim=axis))(k_cache, rep, axis=1)
    v = (lambda x, n, axis: torch.repeat_interleave(x, n, dim=axis))(v_cache, rep, axis=1)
    scores = torch.einsum("bhqd,bhkd->bhqk", q, k) * (Dh ** -0.5)
    p = _softmax(scores, axis=-1)
    o = torch.einsum("bhqk,bhkd->bhqd", p, v)
    return (lambda x, *d: x.permute(*d))(o, 0, 2, 1, 3).reshape(B, Sq, Hq * Dh) @ wo
```

```cpp
#include <hip/hip_runtime.h>
#include <hip/hip_bf16.h>
#include <stdint.h>

typedef short bf16x8 __attribute__((ext_vector_type(8)));
typedef short bf16x4 __attribute__((ext_vector_type(4)));
typedef float f32x4 __attribute__((ext_vector_type(4)));
typedef float f32x2 __attribute__((ext_vector_type(2)));
typedef __bf16 bf2 __attribute__((ext_vector_type(2)));
typedef unsigned u32x2 __attribute__((ext_vector_type(2)));
typedef unsigned u32x4 __attribute__((ext_vector_type(4)));
#define LAS __attribute__((address_space(3)))
#define DI __device__ __forceinline__

constexpr int NB = 16, SQ = 48, DM = 512, HQ = 32, HKV = 8, SK = 512, DH = 128;
constexpr unsigned XOFF = 0;
constexpr unsigned ROFF = 49152;
constexpr unsigned SLOT = 32768;
constexpr unsigned MOFF = ROFF + 3 * SLOT;
constexpr unsigned LDS_BYTES = 163840;

DI unsigned rfl(unsigned x) { return __builtin_amdgcn_readfirstlane(x); }
DI unsigned cvtpk(float lo, float hi) {
  f32x2 v = {lo, hi};
  bf2 b = __builtin_convertvector(v, bf2);
  return __builtin_bit_cast(unsigned, b);
}
#define BARRIER() asm volatile("s_waitcnt lgkmcnt(0)\n\ts_barrier" ::: "memory")
#define WAITV(n) asm volatile("s_waitcnt vmcnt(" #n ")" ::: "memory")

DI void dma(__amdgpu_buffer_rsrc_t rsrc, unsigned voff, unsigned soff, unsigned ldsdst) {
  asm volatile("s_nop 4\n\ts_mov_b32 m0, %0\n\ts_nop 0\n\tbuffer_load_dwordx4 %1, %2, %3 offen lds"
               :: "s"(ldsdst), "v"(voff), "s"(rsrc), "s"(soff) : "memory");
}
DI bf16x8 lds_b128(unsigned a) { return *(const LAS bf16x8*)(uintptr_t)a; }
DI bf16x4 lds_tr(unsigned a) { return __builtin_amdgcn_ds_read_tr16_b64_v4i16((LAS bf16x4*)(uintptr_t)a); }
DI bf16x8 cat(bf16x4 a, bf16x4 b) { return __builtin_shufflevector(a, b, 0, 1, 2, 3, 4, 5, 6, 7); }
DI f32x4 mfma(bf16x8 a, bf16x8 b, f32x4 c) { return __builtin_amdgcn_mfma_f32_16x16x32_bf16(a, b, c, 0, 0, 0); }
DI unsigned fsw(unsigned row) { return (row & 3u) | (((row >> 3) & 1u) << 2); }

__global__ __launch_bounds__(512, 2)
void gqa_fused(const unsigned short* __restrict__ x, const unsigned short* __restrict__ kc,
               const unsigned short* __restrict__ vc, const unsigned short* __restrict__ wq,
               const unsigned short* __restrict__ wo, float* __restrict__ part) {
  extern __shared__ __attribute__((aligned(16))) char smem[];
  const unsigned lb = (unsigned)(uintptr_t)(LAS char*)smem;
  const unsigned tid = threadIdx.x, lane = tid & 63u;
  const unsigned w = rfl(tid >> 6);
  const unsigned bid = blockIdx.x, hkv = bid & 7u, idx = bid >> 3, b = idx >> 1, half = idx & 1u;
  const unsigned h0 = hkv * 4u + half * 2u;
  const unsigned g = lane >> 4, li = lane & 15u, q4 = li >> 2, p4 = lane & 3u;

  const __amdgpu_buffer_rsrc_t rx = __builtin_amdgcn_make_buffer_rsrc((void*)(x + (size_t)b * SQ * DM), (short)0, SQ * DM * 2, 0x00020000);
  const __amdgpu_buffer_rsrc_t rwq = __builtin_amdgcn_make_buffer_rsrc((void*)wq, (short)0, DM * HQ * DH * 2, 0x00020000);
  const __amdgpu_buffer_rsrc_t rwo = __builtin_amdgcn_make_buffer_rsrc((void*)wo, (short)0, DM * HQ * DH * 2, 0x00020000);
  const __amdgpu_buffer_rsrc_t rk = __builtin_amdgcn_make_buffer_rsrc((void*)(kc + ((size_t)b * HKV + hkv) * SK * DH), (short)0, SK * DH * 2, 0x00020000);
  const __amdgpu_buffer_rsrc_t rv = __builtin_amdgcn_make_buffer_rsrc((void*)(vc + ((size_t)b * HKV + hkv) * SK * DH), (short)0, SK * DH * 2, 0x00020000);

  auto issue = [&](int s) {
    const unsigned dst = lb + ROFF + (unsigned)(s % 3) * SLOT + w * 4096u;
    if (s < 8) {
#pragma unroll
      for (int i = 0; i < 4; ++i) {
        const unsigned row = w * 8u + 2u * i + (lane >> 5);
        const unsigned c = (lane & 31u) ^ (fsw(row) << 1);
        dma(rwq, row * 8192u + c * 16u, (unsigned)s * 64u * 8192u + h0 * 256u, dst + 1024u * i);
      }
    } else if (s < 12) {
#pragma unroll
      for (int i = 0; i < 4; ++i) {
        const unsigned row = w * 16u + 4u * i + g;
        const unsigned c = li ^ (row & 15u);
        dma(rk, row * 256u + c * 16u, (unsigned)(s - 8) * 32768u, dst + 1024u * i);
      }
    } else if (s < 16) {
#pragma unroll
      for (int i = 0; i < 4; ++i) {
        const unsigned row = w * 16u + 4u * i + g;
        const unsigned c = li ^ ((row & 7u) << 1);
        dma(rv, row * 256u + c * 16u, (unsigned)(s - 12) * 32768u, dst + 1024u * i);
      }
    } else {
#pragma unroll
      for (int i = 0; i < 4; ++i) {
        const unsigned row = w * 4u + i;
        const unsigned c = lane ^ (fsw(row) << 1);
        dma(rwo, row * 1024u + c * 16u, (h0 * 128u + (unsigned)(s - 16) * 32u) * 1024u, dst + 1024u * i);
      }
    }
  };
#define STEP_TOP(s) do { if ((s) < 23) WAITV(4); else WAITV(0); BARRIER(); if ((s) + 2 < 24) issue((s) + 2); } while (0)

#pragma unroll
  for (int i = 0; i < 6; ++i) {
    const unsigned row = w * 6u + i;
    const unsigned c = lane ^ (row & 15u);
    dma(rx, row * 1024u + c * 16u, 0u, lb + XOFF + row * 1024u);
  }
  issue(0);
  issue(1);

  f32x4 qa[2][3];
#pragma unroll
  for (int t = 0; t < 2; ++t)
#pragma unroll
    for (int j = 0; j < 3; ++j) qa[t][j] = f32x4{0.f, 0.f, 0.f, 0.f};
#pragma unroll
  for (int s = 0; s < 8; ++s) {
    STEP_TOP(s);
    const unsigned sb = lb + ROFF + (unsigned)(s % 3) * SLOT;
#pragma unroll
    for (int ks = 0; ks < 2; ++ks) {
      bf16x8 a[2], bx[3];
#pragma unroll
      for (int t = 0; t < 2; ++t) {
        bf16x4 h[2];
#pragma unroll
        for (int u = 0; u < 2; ++u) {
          const unsigned row = 32u * ks + 8u * g + 4u * u + q4;
          const unsigned ch = (4u * w + 2u * t + (p4 >> 1)) ^ (fsw(row) << 1);
          h[u] = lds_tr(sb + row * 512u + ch * 16u + 8u * (p4 & 1u));
        }
        a[t] = cat(h[0], h[1]);
      }
#pragma unroll
      for (int j = 0; j < 3; ++j) {
        const unsigned row = 16u * j + li;
        const unsigned ch = (8u * s + 4u * ks + g) ^ li;
        bx[j] = lds_b128(lb + XOFF + row * 1024u + ch * 16u);
      }
#pragma unroll
      for (int t = 0; t < 2; ++t)
#pragma unroll
        for (int j = 0; j < 3; ++j) qa[t][j] = mfma(a[t], bx[j], qa[t][j]);
    }
  }
  BARRIER();
  {
    const unsigned r = w >> 2;
#pragma unroll
    for (int t = 0; t < 2; ++t)
#pragma unroll
      for (int j = 0; j < 3; ++j) {
        const unsigned row = r * 48u + 16u * j + li;
        const unsigned d = 32u * (w & 3u) + 16u * t + 4u * g;
        u32x2 v = {cvtpk(qa[t][j][0], qa[t][j][1]), cvtpk(qa[t][j][2], qa[t][j][3])};
        *(LAS u32x2*)(uintptr_t)(lb + XOFF + row * 256u + (((d >> 3) ^ (row & 15u)) * 16u) + (d & 7u) * 2u) = v;
      }
  }
  BARRIER();
  const unsigned r = w >> 2, c = w & 3u;
  bf16x8 qf[3][4];
#pragma unroll
  for (int j = 0; j < 3; ++j)
#pragma unroll
    for (int s = 0; s < 4; ++s) {
      const unsigned row = r * 48u + 16u * j + li;
      qf[j][s] = lds_b128(lb + XOFF + row * 256u + (((4u * s + g) ^ (row & 15u)) * 16u));
    }

  f32x4 sc[4][2][3];
#pragma unroll
  for (int js = 0; js < 4; ++js) {
    STEP_TOP(8 + js);
    const unsigned sb = lb + ROFF + (unsigned)((8 + js) % 3) * SLOT;
#pragma unroll
    for (int kt = 0; kt < 2; ++kt) {
#pragma unroll
      for (int j = 0; j < 3; ++j) sc[js][kt][j] = f32x4{0.f, 0.f, 0.f, 0.f};
      const unsigned key = 32u * c + 16u * kt + li;
#pragma unroll
      for (int s = 0; s < 4; ++s) {
        const bf16x8 a = lds_b128(sb + key * 256u + (((4u * s + g) ^ li) * 16u));
#pragma unroll
        for (int j = 0; j < 3; ++j) sc[js][kt][j] = mfma(a, qf[j][s], sc[js][kt][j]);
      }
    }
  }
  const float CS = 0.088388347648318447f * 1.4426950408889634f;
  LAS float* smax = (LAS float*)(uintptr_t)(lb + MOFF);
  LAS float* ssum = (LAS float*)(uintptr_t)(lb + MOFF + 2048u);
  float mx[3];
#pragma unroll
  for (int j = 0; j < 3; ++j) {
    float m = sc[0][0][j][0];
#pragma unroll
    for (int js = 0; js < 4; ++js)
#pragma unroll
      for (int kt = 0; kt < 2; ++kt)
#pragma unroll
        for (int e = 0; e < 4; ++e) m = fmaxf(m, sc[js][kt][j][e]);
    m = fmaxf(m, __shfl_xor(m, 16));
    m = fmaxf(m, __shfl_xor(m, 32));
    if (g == 0) smax[(r * 4u + c) * 48u + 16u * j + li] = m;
  }
  BARRIER();
#pragma unroll
  for (int j = 0; j < 3; ++j) {
    float m = smax[(r * 4u + 0u) * 48u + 16u * j + li];
    m = fmaxf(m, smax[(r * 4u + 1u) * 48u + 16u * j + li]);
    m = fmaxf(m, smax[(r * 4u + 2u) * 48u + 16u * j + li]);
    m = fmaxf(m, smax[(r * 4u + 3u) * 48u + 16u * j + li]);
    mx[j] = -m * CS;
  }
  bf16x8 pf[4][3];
#pragma unroll
  for (int j = 0; j < 3; ++j) {
    float l = 0.f;
#pragma unroll
    for (int js = 0; js < 4; ++js) {
      float pv[8];
#pragma unroll
      for (int kt = 0; kt < 2; ++kt)
#pragma unroll
        for (int e = 0; e < 4; ++e) {
          const float pe = __builtin_amdgcn_exp2f(fmaf(sc[js][kt][j][e], CS, mx[j]));
          pv[4 * kt + e] = pe;
          l += pe;
        }
      u32x4 pk = {cvtpk(pv[0], pv[1]), cvtpk(pv[2], pv[3]), cvtpk(pv[4], pv[5]), cvtpk(pv[6], pv[7])};
      pf[js][j] = __builtin_bit_cast(bf16x8, pk);
    }
    l += __shfl_xor(l, 16);
    l += __shfl_xor(l, 32);
    if (g == 0) ssum[(r * 4u + c) * 48u + 16u * j + li] = l;
  }

  f32x4 oa[8][3];
#pragma unroll
  for (int dt = 0; dt < 8; ++dt)
#pragma unroll
    for (int j = 0; j < 3; ++j) oa[dt][j] = f32x4{0.f, 0.f, 0.f, 0.f};
#pragma unroll
  for (int js = 0; js < 4; ++js) {
    STEP_TOP(12 + js);
    const unsigned sb = lb + ROFF + (unsigned)((12 + js) % 3) * SLOT;
#pragma unroll
    for (int dt = 0; dt < 8; ++dt) {
      bf16x4 h[2];
#pragma unroll
      for (int u = 0; u < 2; ++u) {
        const unsigned row = 32u * c + 16u * u + 4u * g + q4;
        const unsigned ch = (2u * dt + (p4 >> 1)) ^ ((row & 7u) << 1);
        h[u] = lds_tr(sb + row * 256u + ch * 16u + 8u * (p4 & 1u));
      }
      const bf16x8 a = cat(h[0], h[1]);
#pragma unroll
      for (int j = 0; j < 3; ++j) oa[dt][j] = mfma(a, pf[js][j], oa[dt][j]);
    }
  }
  f32x4 of_[2][3];
#pragma unroll
  for (int cc = 0; cc < 4; ++cc)
    if (c == (unsigned)cc) {
#pragma unroll
      for (int t = 0; t < 2; ++t)
#pragma unroll
        for (int j = 0; j < 3; ++j) of_[t][j] = oa[2 * cc + t][j];
    }
#pragma unroll
  for (int rd = 1; rd < 4; ++rd) {
    const unsigned dstc = (c + rd) & 3u;
    const unsigned wb = lb + XOFF + (r * 4u + dstc) * 6144u + lane * 16u;
#pragma unroll
    for (int cc = 0; cc < 4; ++cc)
      if (dstc == (unsigned)cc) {
#pragma unroll
        for (int t = 0; t < 2; ++t)
#pragma unroll
          for (int j = 0; j < 3; ++j) *(LAS f32x4*)(uintptr_t)(wb + (t * 3 + j) * 1024u) = oa[2 * cc + t][j];
      }
    BARRIER();
    const unsigned rb = lb + XOFF + (r * 4u + c) * 6144u + lane * 16u;
#pragma unroll
    for (int t = 0; t < 2; ++t)
#pragma unroll
      for (int j = 0; j < 3; ++j) of_[t][j] += *(const LAS f32x4*)(uintptr_t)(rb + (t * 3 + j) * 1024u);
    BARRIER();
  }
#pragma unroll
  for (int j = 0; j < 3; ++j) {
    float l = ssum[(r * 4u + 0u) * 48u + 16u * j + li] + ssum[(r * 4u + 1u) * 48u + 16u * j + li] +
              ssum[(r * 4u + 2u) * 48u + 16u * j + li] + ssum[(r * 4u + 3u) * 48u + 16u * j + li];
    const float inv = 1.0f / l;
#pragma unroll
    for (int t = 0; t < 2; ++t) {
      const unsigned row = 16u * j + li;
      const unsigned k = r * 128u + 32u * c + 16u * t + 4u * g;
      u32x2 v = {cvtpk(of_[t][j][0] * inv, of_[t][j][1] * inv), cvtpk(of_[t][j][2] * inv, of_[t][j][3] * inv)};
      *(LAS u32x2*)(uintptr_t)(lb + XOFF + row * 512u + (((k >> 3) ^ li) * 16u) + (k & 7u) * 2u) = v;
    }
  }
  BARRIER();
  bf16x8 of[3][8];
#pragma unroll
  for (int j = 0; j < 3; ++j)
#pragma unroll
    for (int s = 0; s < 8; ++s)
      of[j][s] = lds_b128(lb + XOFF + (16u * j + li) * 512u + (((4u * s + g) ^ li) * 16u));

  f32x4 pa[4][3];
#pragma unroll
  for (int t = 0; t < 4; ++t)
#pragma unroll
    for (int j = 0; j < 3; ++j) pa[t][j] = f32x4{0.f, 0.f, 0.f, 0.f};
#pragma unroll
  for (int s = 0; s < 8; ++s) {
    STEP_TOP(16 + s);
    const unsigned sb = lb + ROFF + (unsigned)((16 + s) % 3) * SLOT;
#pragma unroll
    for (int t = 0; t < 4; ++t) {
      bf16x4 h[2];
#pragma unroll
      for (int u = 0; u < 2; ++u) {
        const unsigned row = 8u * g + 4u * u + q4;
        const unsigned ch = (8u * w + 2u * t + (p4 >> 1)) ^ (fsw(row) << 1);
        h[u] = lds_tr(sb + row * 1024u + ch * 16u + 8u * (p4 & 1u));
      }
      const bf16x8 a = cat(h[0], h[1]);
#pragma unroll
      for (int j = 0; j < 3; ++j) pa[t][j] = mfma(a, of[j][s], pa[t][j]);
    }
  }
  float* pp = part + ((size_t)(b * 16u + hkv * 2u + half)) * (SQ * DM);
#pragma unroll
  for (int t = 0; t < 4; ++t)
#pragma unroll
    for (int j = 0; j < 3; ++j)
      *(f32x4*)(pp + (16u * j + li) * 512u + 64u * w + 16u * t + 4u * g) = pa[t][j];
}

__global__ __launch_bounds__(256)
void gqa_reduce(const float* __restrict__ part, unsigned short* __restrict__ out) {
  const unsigned i = blockIdx.x * 256u + threadIdx.x;
  const unsigned b = i / (SQ * DM / 4), e = i % (SQ * DM / 4);
  const f32x4* p = (const f32x4*)part + (size_t)b * 16u * (SQ * DM / 4) + e;
  f32x4 v[16];
#pragma unroll
  for (int h = 0; h < 16; ++h) v[h] = p[(size_t)h * (SQ * DM / 4)];
  f32x4 s = v[0];
#pragma unroll
  for (int h = 1; h < 16; ++h) s += v[h];
  u32x2 o = {cvtpk(s[0], s[1]), cvtpk(s[2], s[3])};
  *(u32x2*)(out + (size_t)i * 4u) = o;
}

extern "C" void kernel_launch(void* const* d_in, const int* in_sizes, int n_in,
                              void* d_out, int out_size, void* d_ws, size_t ws_size,
                              hipStream_t stream) {
  (void)in_sizes; (void)n_in; (void)out_size; (void)ws_size;
  hipFuncSetAttribute((const void*)gqa_fused, hipFuncAttributeMaxDynamicSharedMemorySize, LDS_BYTES);
  const unsigned short* x = (const unsigned short*)d_in[0];
  const unsigned short* kc = (const unsigned short*)d_in[1];
  const unsigned short* vc = (const unsigned short*)d_in[2];
  const unsigned short* wq = (const unsigned short*)d_in[3];
  const unsigned short* wo = (const unsigned short*)d_in[4];
  float* part = (float*)d_ws;
  hipLaunchKernelGGL(gqa_fused, dim3(256), dim3(512), LDS_BYTES, stream, x, kc, vc, wq, wo, part);
  hipLaunchKernelGGL(gqa_reduce, dim3(NB * SQ * DM / 4 / 256), dim3(256), 0, stream, (const float*)part, (unsigned short*)d_out);
}
```

```cpp
#define ST_AUX 16
#include <hip/hip_runtime.h>
#include <hip/hip_bf16.h>
#include <stdint.h>

typedef short bf16x8 __attribute__((ext_vector_type(8)));
typedef short bf16x4 __attribute__((ext_vector_type(4)));
typedef float f32x4 __attribute__((ext_vector_type(4)));
typedef float f32x2 __attribute__((ext_vector_type(2)));
typedef __bf16 bf2 __attribute__((ext_vector_type(2)));
typedef unsigned u32x2 __attribute__((ext_vector_type(2)));
typedef unsigned u32x4 __attribute__((ext_vector_type(4)));
#define LAS __attribute__((address_space(3)))
#define DI __device__ __forceinline__

constexpr int NB = 16, SQ = 48, DM = 512, HQ = 32, HKV = 8, SK = 512, DH = 128;
constexpr unsigned XOFF = 0;
constexpr unsigned ROFF = 49152;
constexpr unsigned SLOT = 32768;
constexpr unsigned MOFF = ROFF + 3 * SLOT;
constexpr unsigned LDS_BYTES = 163840;

DI unsigned rfl(unsigned x) { return __builtin_amdgcn_readfirstlane(x); }
DI unsigned cvtpk(float lo, float hi) {
  f32x2 v = {lo, hi};
  bf2 b = __builtin_convertvector(v, bf2);
  return __builtin_bit_cast(unsigned, b);
}
#define BARRIER() asm volatile("s_waitcnt lgkmcnt(0)\n\ts_barrier" ::: "memory")
#define WAITV(n) asm volatile("s_waitcnt vmcnt(" #n ")" ::: "memory")
#define STAMP(k)
#define STAMPRT(k)

#define KV_NT 0
#define LD_AUX 0
DI void dma_nt(__amdgpu_buffer_rsrc_t rsrc, unsigned voff, unsigned soff, unsigned ldsdst) {
  asm volatile("s_nop 4\n\ts_mov_b32 m0, %0\n\ts_nop 0\n\tbuffer_load_dwordx4 %1, %2, %3 offen nt lds"
               :: "s"(ldsdst), "v"(voff), "s"(rsrc), "s"(soff) : "memory");
}
DI void dma(__amdgpu_buffer_rsrc_t rsrc, unsigned voff, unsigned soff, unsigned ldsdst) {
  asm volatile("s_nop 4\n\ts_mov_b32 m0, %0\n\ts_nop 0\n\tbuffer_load_dwordx4 %1, %2, %3 offen lds"
               :: "s"(ldsdst), "v"(voff), "s"(rsrc), "s"(soff) : "memory");
}
DI bf16x8 lds_b128(unsigned a) { return *(const LAS bf16x8*)(uintptr_t)a; }
DI bf16x4 lds_tr(unsigned a) { return __builtin_amdgcn_ds_read_tr16_b64_v4i16((LAS bf16x4*)(uintptr_t)a); }
DI bf16x8 cat(bf16x4 a, bf16x4 b) { return __builtin_shufflevector(a, b, 0, 1, 2, 3, 4, 5, 6, 7); }
DI f32x4 mfma(bf16x8 a, bf16x8 b, f32x4 c) { return __builtin_amdgcn_mfma_f32_16x16x32_bf16(a, b, c, 0, 0, 0); }
DI unsigned fsw(unsigned row) { return (row & 3u) | (((row >> 3) & 1u) << 2); }

__global__ __launch_bounds__(512, 2)
void gqa_fused(const unsigned short* __restrict__ x, const unsigned short* __restrict__ kc,
               const unsigned short* __restrict__ vc, const unsigned short* __restrict__ wq,
               const unsigned short* __restrict__ wo, float* __restrict__ part) {
  extern __shared__ __attribute__((aligned(16))) char smem[];
  const unsigned lb = (unsigned)(uintptr_t)(LAS char*)smem;
  const unsigned tid = threadIdx.x, lane = tid & 63u;
  const unsigned w = rfl(tid >> 6);
  const unsigned bid = blockIdx.x, hkv = bid & 7u, idx = bid >> 3, b = idx >> 1, half = idx & 1u;
  const unsigned h0 = hkv * 4u + half * 2u;
  const unsigned g = lane >> 4, li = lane & 15u, q4 = li >> 2, p4 = lane & 3u;

  const __amdgpu_buffer_rsrc_t rx = __builtin_amdgcn_make_buffer_rsrc((void*)(x + (size_t)b * SQ * DM), (short)0, SQ * DM * 2, 0x00020000);
  const __amdgpu_buffer_rsrc_t rwq = __builtin_amdgcn_make_buffer_rsrc((void*)wq, (short)0, DM * HQ * DH * 2, 0x00020000);
  const __amdgpu_buffer_rsrc_t rwo = __builtin_amdgcn_make_buffer_rsrc((void*)wo, (short)0, DM * HQ * DH * 2, 0x00020000);
  const __amdgpu_buffer_rsrc_t rk = __builtin_amdgcn_make_buffer_rsrc((void*)(kc + ((size_t)b * HKV + hkv) * SK * DH), (short)0, SK * DH * 2, 0x00020000);
  const __amdgpu_buffer_rsrc_t rv = __builtin_amdgcn_make_buffer_rsrc((void*)(vc + ((size_t)b * HKV + hkv) * SK * DH), (short)0, SK * DH * 2, 0x00020000);

  auto issue = [&](int s) {
    const unsigned dst = lb + ROFF + (unsigned)(s % 3) * SLOT + w * 4096u;
    if (s < 8) {
#pragma unroll
      for (int i = 0; i < 4; ++i) {
        const unsigned row = w * 8u + 2u * i + (lane >> 5);
        const unsigned c = (lane & 31u) ^ (fsw(row) << 1);
        dma(rwq, row * 8192u + c * 16u, (unsigned)s * 64u * 8192u + h0 * 256u, dst + 1024u * i);
      }
    } else if (s < 12) {
#pragma unroll
      for (int i = 0; i < 4; ++i) {
        const unsigned row = w * 16u + 4u * i + g;
        const unsigned c = li ^ (row & 15u);
        if (KV_NT) dma_nt(rk, row * 256u + c * 16u, (unsigned)(s - 8) * 32768u, dst + 1024u * i); else
        dma(rk, row * 256u + c * 16u, (unsigned)(s - 8) * 32768u, dst + 1024u * i);
      }
    } else if (s < 16) {
#pragma unroll
      for (int i = 0; i < 4; ++i) {
        const unsigned row = w * 16u + 4u * i + g;
        const unsigned c = li ^ ((row & 7u) << 1);
        if (KV_NT) dma_nt(rv, row * 256u + c * 16u, (unsigned)(s - 12) * 32768u, dst + 1024u * i); else
        dma(rv, row * 256u + c * 16u, (unsigned)(s - 12) * 32768u, dst + 1024u * i);
      }
    } else {
#pragma unroll
      for (int i = 0; i < 4; ++i) {
        const unsigned row = w * 4u + i;
        const unsigned c = lane ^ (fsw(row) << 1);
        dma(rwo, row * 1024u + c * 16u, (h0 * 128u + (unsigned)(s - 16) * 32u) * 1024u, dst + 1024u * i);
      }
    }
  };
#define STEP_TOP(s) do { if ((s) < 23) WAITV(4); else WAITV(0); BARRIER(); STAMP(1 + (s) + ((s) >= 8) + ((s) >= 12) + 2 * ((s) >= 16)); if ((s) + 2 < 24) issue((s) + 2); } while (0)

  STAMPRT(NSTAMP - 2);
  STAMP(0);
#pragma unroll
  for (int i = 0; i < 6; ++i) {
    const unsigned row = w * 6u + i;
    const unsigned c = lane ^ (row & 15u);
    dma(rx, row * 1024u + c * 16u, 0u, lb + XOFF + row * 1024u);
  }
  issue(0);
  issue(1);

  f32x4 qa[2][3];
#pragma unroll
  for (int t = 0; t < 2; ++t)
#pragma unroll
    for (int j = 0; j < 3; ++j) qa[t][j] = f32x4{0.f, 0.f, 0.f, 0.f};
#pragma unroll
  for (int s = 0; s < 8; ++s) {
    STEP_TOP(s);
    const unsigned sb = lb + ROFF + (unsigned)(s % 3) * SLOT;
#pragma unroll
    for (int ks = 0; ks < 2; ++ks) {
      bf16x8 a[2], bx[3];
#pragma unroll
      for (int t = 0; t < 2; ++t) {
        bf16x4 h[2];
#pragma unroll
        for (int u = 0; u < 2; ++u) {
          const unsigned row = 32u * ks + 8u * g + 4u * u + q4;
          const unsigned ch = (4u * w + 2u * t + (p4 >> 1)) ^ (fsw(row) << 1);
          h[u] = lds_tr(sb + row * 512u + ch * 16u + 8u * (p4 & 1u));
        }
        a[t] = cat(h[0], h[1]);
      }
#pragma unroll
      for (int j = 0; j < 3; ++j) {
        const unsigned row = 16u * j + li;
        const unsigned ch = (8u * s + 4u * ks + g) ^ li;
        bx[j] = lds_b128(lb + XOFF + row * 1024u + ch * 16u);
      }
#pragma unroll
      for (int t = 0; t < 2; ++t)
#pragma unroll
        for (int j = 0; j < 3; ++j) qa[t][j] = mfma(a[t], bx[j], qa[t][j]);
    }
  }
  BARRIER();
  STAMP(9);
  {
    const unsigned r = w >> 2;
#pragma unroll
    for (int t = 0; t < 2; ++t)
#pragma unroll
      for (int j = 0; j < 3; ++j) {
        const unsigned row = r * 48u + 16u * j + li;
        const unsigned d = 32u * (w & 3u) + 16u * t + 4u * g;
        u32x2 v = {cvtpk(qa[t][j][0], qa[t][j][1]), cvtpk(qa[t][j][2], qa[t][j][3])};
        *(LAS u32x2*)(uintptr_t)(lb + XOFF + row * 256u + (((d >> 3) ^ (row & 15u)) * 16u) + (d & 7u) * 2u) = v;
      }
  }
  BARRIER();
  const unsigned r = w >> 2, c = w & 3u;
  bf16x8 qf[3][4];
#pragma unroll
  for (int j = 0; j < 3; ++j)
#pragma unroll
    for (int s = 0; s < 4; ++s) {
      const unsigned row = r * 48u + 16u * j + li;
      qf[j][s] = lds_b128(lb + XOFF + row * 256u + (((4u * s + g) ^ (row & 15u)) * 16u));
    }

  f32x4 sc[4][2][3];
#pragma unroll
  for (int js = 0; js < 4; ++js) {
    STEP_TOP(8 + js);
    const unsigned sb = lb + ROFF + (unsigned)((8 + js) % 3) * SLOT;
#pragma unroll
    for (int kt = 0; kt < 2; ++kt) {
#pragma unroll
      for (int j = 0; j < 3; ++j) sc[js][kt][j] = f32x4{0.f, 0.f, 0.f, 0.f};
      const unsigned key = 32u * c + 16u * kt + li;
#pragma unroll
      for (int s = 0; s < 4; ++s) {
        const bf16x8 a = lds_b128(sb + key * 256u + (((4u * s + g) ^ li) * 16u));
#pragma unroll
        for (int j = 0; j < 3; ++j) sc[js][kt][j] = mfma(a, qf[j][s], sc[js][kt][j]);
      }
    }
  }
  STAMP(14);
  const float CS = 0.088388347648318447f * 1.4426950408889634f;
  LAS float* smax = (LAS float*)(uintptr_t)(lb + MOFF);
  LAS float* ssum = (LAS float*)(uintptr_t)(lb + MOFF + 2048u);
  float mx[3];
#pragma unroll
  for (int j = 0; j < 3; ++j) {
    float m = sc[0][0][j][0];
#pragma unroll
    for (int js = 0; js < 4; ++js)
#pragma unroll
      for (int kt = 0; kt < 2; ++kt)
#pragma unroll
        for (int e = 0; e < 4; ++e) m = fmaxf(m, sc[js][kt][j][e]);
    m = fmaxf(m, __shfl_xor(m, 16));
    m = fmaxf(m, __shfl_xor(m, 32));
    if (g == 0) smax[(r * 4u + c) * 48u + 16u * j + li] = m;
  }
  BARRIER();
#pragma unroll
  for (int j = 0; j < 3; ++j) {
    float m = smax[(r * 4u + 0u) * 48u + 16u * j + li];
    m = fmaxf(m, smax[(r * 4u + 1u) * 48u + 16u * j + li]);
    m = fmaxf(m, smax[(r * 4u + 2u) * 48u + 16u * j + li]);
    m = fmaxf(m, smax[(r * 4u + 3u) * 48u + 16u * j + li]);
    mx[j] = -m * CS;
  }
  bf16x8 pf[4][3];
#pragma unroll
  for (int j = 0; j < 3; ++j) {
    float l = 0.f;
#pragma unroll
    for (int js = 0; js < 4; ++js) {
      float pv[8];
#pragma unroll
      for (int kt = 0; kt < 2; ++kt)
#pragma unroll
        for (int e = 0; e < 4; ++e) {
          const float pe = __builtin_amdgcn_exp2f(fmaf(sc[js][kt][j][e], CS, mx[j]));
          pv[4 * kt + e] = pe;
          l += pe;
        }
      u32x4 pk = {cvtpk(pv[0], pv[1]), cvtpk(pv[2], pv[3]), cvtpk(pv[4], pv[5]), cvtpk(pv[6], pv[7])};
      pf[js][j] = __builtin_bit_cast(bf16x8, pk);
    }
    l += __shfl_xor(l, 16);
    l += __shfl_xor(l, 32);
    if (g == 0) ssum[(r * 4u + c) * 48u + 16u * j + li] = l;
  }

  f32x4 oa[8][3];
#pragma unroll
  for (int dt = 0; dt < 8; ++dt)
#pragma unroll
    for (int j = 0; j < 3; ++j) oa[dt][j] = f32x4{0.f, 0.f, 0.f, 0.f};
#pragma unroll
  for (int js = 0; js < 4; ++js) {
    STEP_TOP(12 + js);
    const unsigned sb = lb + ROFF + (unsigned)((12 + js) % 3) * SLOT;
#pragma unroll
    for (int dt = 0; dt < 8; ++dt) {
      bf16x4 h[2];
#pragma unroll
      for (int u = 0; u < 2; ++u) {
        const unsigned row = 32u * c + 16u * u + 4u * g + q4;
        const unsigned ch = (2u * dt + (p4 >> 1)) ^ ((row & 7u) << 1);
        h[u] = lds_tr(sb + row * 256u + ch * 16u + 8u * (p4 & 1u));
      }
      const bf16x8 a = cat(h[0], h[1]);
#pragma unroll
      for (int j = 0; j < 3; ++j) oa[dt][j] = mfma(a, pf[js][j], oa[dt][j]);
    }
  }
  STAMP(19);
  f32x4 of_[2][3];
#pragma unroll
  for (int cc = 0; cc < 4; ++cc)
    if (c == (unsigned)cc) {
#pragma unroll
      for (int t = 0; t < 2; ++t)
#pragma unroll
        for (int j = 0; j < 3; ++j) of_[t][j] = oa[2 * cc + t][j];
    }
#pragma unroll
  for (int rd = 1; rd < 4; ++rd) {
    const unsigned dstc = (c + rd) & 3u;
    const unsigned wb = lb + XOFF + (r * 4u + dstc) * 6144u + lane * 16u;
#pragma unroll
    for (int cc = 0; cc < 4; ++cc)
      if (dstc == (unsigned)cc) {
#pragma unroll
        for (int t = 0; t < 2; ++t)
#pragma unroll
          for (int j = 0; j < 3; ++j) *(LAS f32x4*)(uintptr_t)(wb + (t * 3 + j) * 1024u) = oa[2 * cc + t][j];
      }
    BARRIER();
    const unsigned rb = lb + XOFF + (r * 4u + c) * 6144u + lane * 16u;
#pragma unroll
    for (int t = 0; t < 2; ++t)
#pragma unroll
      for (int j = 0; j < 3; ++j) of_[t][j] += *(const LAS f32x4*)(uintptr_t)(rb + (t * 3 + j) * 1024u);
    BARRIER();
  }
  STAMP(20);
#pragma unroll
  for (int j = 0; j < 3; ++j) {
    float l = ssum[(r * 4u + 0u) * 48u + 16u * j + li] + ssum[(r * 4u + 1u) * 48u + 16u * j + li] +
              ssum[(r * 4u + 2u) * 48u + 16u * j + li] + ssum[(r * 4u + 3u) * 48u + 16u * j + li];
    const float inv = 1.0f / l;
#pragma unroll
    for (int t = 0; t < 2; ++t) {
      const unsigned row = 16u * j + li;
      const unsigned k = r * 128u + 32u * c + 16u * t + 4u * g;
      u32x2 v = {cvtpk(of_[t][j][0] * inv, of_[t][j][1] * inv), cvtpk(of_[t][j][2] * inv, of_[t][j][3] * inv)};
      *(LAS u32x2*)(uintptr_t)(lb + XOFF + row * 512u + (((k >> 3) ^ li) * 16u) + (k & 7u) * 2u) = v;
    }
  }
  BARRIER();
  bf16x8 of[3][8];
#pragma unroll
  for (int j = 0; j < 3; ++j)
#pragma unroll
    for (int s = 0; s < 8; ++s)
      of[j][s] = lds_b128(lb + XOFF + (16u * j + li) * 512u + (((4u * s + g) ^ li) * 16u));

  f32x4 pa[4][3];
#pragma unroll
  for (int t = 0; t < 4; ++t)
#pragma unroll
    for (int j = 0; j < 3; ++j) pa[t][j] = f32x4{0.f, 0.f, 0.f, 0.f};
#pragma unroll
  for (int s = 0; s < 8; ++s) {
    STEP_TOP(16 + s);
    const unsigned sb = lb + ROFF + (unsigned)((16 + s) % 3) * SLOT;
#pragma unroll
    for (int t = 0; t < 4; ++t) {
      bf16x4 h[2];
#pragma unroll
      for (int u = 0; u < 2; ++u) {
        const unsigned row = 8u * g + 4u * u + q4;
        const unsigned ch = (8u * w + 2u * t + (p4 >> 1)) ^ (fsw(row) << 1);
        h[u] = lds_tr(sb + row * 1024u + ch * 16u + 8u * (p4 & 1u));
      }
      const bf16x8 a = cat(h[0], h[1]);
#pragma unroll
      for (int j = 0; j < 3; ++j) pa[t][j] = mfma(a, of[j][s], pa[t][j]);
    }
  }
  float* pp = part + ((size_t)(b * 16u + hkv * 2u + half)) * (SQ * DM);
  const __amdgpu_buffer_rsrc_t rp = __builtin_amdgcn_make_buffer_rsrc((void*)pp, (short)0, SQ * DM * 4, 0x00020000);
#pragma unroll
  for (int t = 0; t < 4; ++t)
#pragma unroll
    for (int j = 0; j < 3; ++j)
      __builtin_amdgcn_raw_buffer_store_b128(__builtin_bit_cast(u32x4, pa[t][j]), rp, ((16u * j + li) * 512u + 64u * w + 16u * t + 4u * g) * 4u, 0, ST_AUX);
}

__global__ __launch_bounds__(256)
void gqa_reduce(const float* __restrict__ part, unsigned short* __restrict__ out) {
  const unsigned i = blockIdx.x * 256u + threadIdx.x;
  const unsigned b = i / (SQ * DM / 4), e = i % (SQ * DM / 4);
  const __amdgpu_buffer_rsrc_t rp = __builtin_amdgcn_make_buffer_rsrc((void*)part, (short)0, 256 * SQ * DM * 4, 0x00020000);
  f32x4 v[16];
#pragma unroll
  for (int h = 0; h < 16; ++h) v[h] = __builtin_bit_cast(f32x4, __builtin_amdgcn_raw_buffer_load_b128(rp, (b * 16u * (SQ * DM / 4) + e) * 16u, h * (SQ * DM * 4), LD_AUX));
  f32x4 s = v[0];
#pragma unroll
  for (int h = 1; h < 16; ++h) s += v[h];
  u32x2 o = {cvtpk(s[0], s[1]), cvtpk(s[2], s[3])};
  *(u32x2*)(out + (size_t)i * 4u) = o;
}

extern "C" void kernel_launch(void* const* d_in, const int* in_sizes, int n_in,
                              void* d_out, int out_size, void* d_ws, size_t ws_size,
                              hipStream_t stream) {
  (void)in_sizes; (void)n_in; (void)out_size; (void)ws_size;
  hipFuncSetAttribute((const void*)gqa_fused, hipFuncAttributeMaxDynamicSharedMemorySize, LDS_BYTES);
  const unsigned short* x = (const unsigned short*)d_in[0];
  const unsigned short* kc = (const unsigned short*)d_in[1];
  const unsigned short* vc = (const unsigned short*)d_in[2];
  const unsigned short* wq = (const unsigned short*)d_in[3];
  const unsigned short* wo = (const unsigned short*)d_in[4];
  float* part = (float*)d_ws;
  hipLaunchKernelGGL(gqa_fused, dim3(256), dim3(512), LDS_BYTES, stream, x, kc, vc, wq, wo, part);
  hipLaunchKernelGGL(gqa_reduce, dim3(NB * SQ * DM / 4 / 256), dim3(256), 0, stream, (const float*)part, (unsigned short*)d_out);
}
```

```cpp
#include <hip/hip_runtime.h>
#include <hip/hip_bf16.h>
#include <stdint.h>

typedef short bf16x8 __attribute__((ext_vector_type(8)));
typedef short bf16x4 __attribute__((ext_vector_type(4)));
typedef float f32x4 __attribute__((ext_vector_type(4)));
typedef float f32x2 __attribute__((ext_vector_type(2)));
typedef __bf16 bf2 __attribute__((ext_vector_type(2)));
typedef unsigned u32x2 __attribute__((ext_vector_type(2)));
typedef unsigned u32x4 __attribute__((ext_vector_type(4)));
#define LAS __attribute__((address_space(3)))
#define DI __device__ __forceinline__

constexpr int NB = 16, SQ = 48, DM = 512, HQ = 32, HKV = 8, SK = 512, DH = 128;
constexpr unsigned XOFF = 0;
constexpr unsigned ROFF = 65536;
constexpr unsigned SLOT = 32768;
constexpr unsigned PBUF = 24576;
constexpr unsigned SMAXOFF = 49152, SSUMOFF = 51200;
constexpr unsigned LDS_BYTES = 163840;

DI unsigned rfl(unsigned x) { return __builtin_amdgcn_readfirstlane(x); }
DI unsigned cvtpk(float lo, float hi) {
  f32x2 v = {lo, hi};
  bf2 b = __builtin_convertvector(v, bf2);
  return __builtin_bit_cast(unsigned, b);
}
#define BARRIER() asm volatile("s_waitcnt lgkmcnt(0)\n\ts_barrier" ::: "memory")
#define SB() __builtin_amdgcn_sched_barrier(0)
template <int N> DI void waitv() { asm volatile("s_waitcnt vmcnt(%0)" :: "i"(N > 63 ? 63 : N) : "memory"); }
#define STAMP(k)
#define FSTAMP(k)
#define STAMPRT(k)

template <int NT> DI void dma(__amdgpu_buffer_rsrc_t rsrc, unsigned voff, unsigned soff, unsigned ldsdst) {
  if (NT) asm volatile("s_mov_b32 m0, %0\n\ts_nop 0\n\tbuffer_load_dwordx4 %1, %2, %3 offen nt lds"
               :: "s"(ldsdst), "v"(voff), "s"(rsrc), "s"(soff) : "memory");
  else asm volatile("s_mov_b32 m0, %0\n\ts_nop 0\n\tbuffer_load_dwordx4 %1, %2, %3 offen lds"
               :: "s"(ldsdst), "v"(voff), "s"(rsrc), "s"(soff) : "memory");
}
DI bf16x8 lds_b128(unsigned a) { return *(const LAS bf16x8*)(uintptr_t)a; }
DI bf16x4 lds_tr(unsigned a) { return __builtin_amdgcn_ds_read_tr16_b64_v4i16((LAS bf16x4*)(uintptr_t)a); }
DI bf16x8 cat(bf16x4 a, bf16x4 b) { return __builtin_shufflevector(a, b, 0, 1, 2, 3, 4, 5, 6, 7); }
DI f32x4 mfma(bf16x8 a, bf16x8 b, f32x4 c) { return __builtin_amdgcn_mfma_f32_16x16x32_bf16(a, b, c, 0, 0, 0); }
DI unsigned fsw(unsigned row) { return (row & 3u) | (((row >> 3) & 1u) << 2); }
DI float max2f(float a, float b) { return __builtin_elementwise_maximum(a, b); }
DI float max3f(float a, float b, float c) { return max2f(max2f(a, b), c); }
DI void lds_fmax(unsigned a, float v) { asm volatile("ds_max_f32 %0, %1" :: "v"(a), "v"(v) : "memory"); }
DI float xmax4(float v) {
  auto a = __builtin_amdgcn_permlane32_swap(__float_as_uint(v), __float_as_uint(v), false, false);
  v = fmaxf(__uint_as_float(a[0]), __uint_as_float(a[1]));
  auto b = __builtin_amdgcn_permlane16_swap(__float_as_uint(v), __float_as_uint(v), false, false);
  return fmaxf(__uint_as_float(b[0]), __uint_as_float(b[1]));
}

__global__ __launch_bounds__(512, 2)
void gqa_fused(const unsigned short* __restrict__ x, const unsigned short* __restrict__ kc,
               const unsigned short* __restrict__ vc, const unsigned short* __restrict__ wq,
               const unsigned short* __restrict__ wo, unsigned short* __restrict__ part) {
  extern __shared__ __attribute__((aligned(16))) char smem[];
  const unsigned lb = (unsigned)(uintptr_t)(LAS char*)smem;
  const unsigned tid = threadIdx.x, lane = tid & 63u;
  const unsigned w = rfl(tid >> 6);
  const unsigned bid = blockIdx.x, hkv = bid & 7u, idx = bid >> 3, b = idx >> 1, half = idx & 1u;
  const unsigned h0 = hkv * 4u + half * 2u;
  const unsigned g = lane >> 4, li = lane & 15u, q4 = li >> 2, p4 = lane & 3u;
  const unsigned r = w >> 2, c = w & 3u;
  const unsigned rot = b & 7u, nrot = b & 3u;
  const bool loader = (r == 0);

  const __amdgpu_buffer_rsrc_t rx = __builtin_amdgcn_make_buffer_rsrc((void*)(x + (size_t)b * SQ * DM), (short)0, SQ * DM * 2, 0x00020000);
  const __amdgpu_buffer_rsrc_t rwq = __builtin_amdgcn_make_buffer_rsrc((void*)wq, (short)0, DM * HQ * DH * 2, 0x00020000);
  const __amdgpu_buffer_rsrc_t rwo = __builtin_amdgcn_make_buffer_rsrc((void*)wo, (short)0, DM * HQ * DH * 2, 0x00020000);
  const __amdgpu_buffer_rsrc_t rk = __builtin_amdgcn_make_buffer_rsrc((void*)(kc + ((size_t)b * HKV + hkv) * SK * DH), (short)0, SK * DH * 2, 0x00020000);
  const __amdgpu_buffer_rsrc_t rv = __builtin_amdgcn_make_buffer_rsrc((void*)(vc + ((size_t)b * HKV + hkv) * SK * DH), (short)0, SK * DH * 2, 0x00020000);
  unsigned short* pp = part + ((size_t)(b * 16u + hkv * 2u + half)) * (SQ * DM);
  const __amdgpu_buffer_rsrc_t rp = __builtin_amdgcn_make_buffer_rsrc((void*)pp, (short)0, SQ * DM * 2, 0x00020000);

  auto piece = [&](int t, unsigned pb, int i) {
    const unsigned dst = lb + ROFF + (unsigned)(t % 3) * SLOT + pb * 1024u + 1024u * i;
    if (t < 8) {
      const unsigned kb = ((unsigned)t + rot) & 7u;
      const unsigned row = 2u * i + (lane >> 5);
      const unsigned ch = (lane & 31u) ^ (fsw(row) << 1);
      dma<0>(rwq, row * 8192u + ch * 16u, (kb * 64u + pb * 2u) * 8192u + h0 * 256u, dst);
    } else if (t < 16) {
      const bool isk = (t == 8) || (t & 1 && t < 15);
      const int blk = t == 8 ? 0 : (t == 15 ? 3 : (isk ? (t - 7) / 2 : (t - 10) / 2));
      const unsigned row = 4u * i + g;
      const unsigned ch = isk ? (li ^ (row & 15u)) : (li ^ ((row & 7u) << 1));
      dma<1>(isk ? rk : rv, row * 256u + ch * 16u, (unsigned)blk * 32768u + pb * 1024u, dst);
    } else {
      const unsigned nb = (((unsigned)(t - 16) >> 1) + nrot) & 3u, kh = (unsigned)(t - 16) & 1u;
      const unsigned row = 4u * i + g;
      const unsigned ch = li ^ (fsw(row) << 1);
      dma<0>(rwo, row * 1024u + ch * 16u, (h0 * 128u + kh * 128u + pb * 4u) * 1024u + nb * 256u, dst);
    }
  };
  auto issue8 = [&](int t) {
#pragma unroll
    for (int i = 0; i < 8; ++i) piece(t, c * 8u, i);
  };

  STAMPRT(NSTAMP - 2);
  STAMP(0);
  if (loader) {
#pragma unroll
    for (int i = 0; i < 12; ++i) {
      const unsigned row = c * 12u + i;
      const unsigned ch = lane ^ (row & 15u);
      dma<0>(rx, ch * 16u, row * 1024u, lb + XOFF + row * 1024u);
    }
    issue8(0); issue8(1); issue8(2);
    if (tid < 96u) *(LAS float*)(uintptr_t)(lb + SMAXOFF + tid * 4u) = -__builtin_inff();
    waitv<16>();
    BARRIER(); STAMP(1);
#define L1STEP(d) do { if ((d) < 7) waitv<8>(); BARRIER(); STAMP(2 + (d)); issue8((d) + 3); } while (0)
    L1STEP(0); L1STEP(1); L1STEP(2); L1STEP(3); L1STEP(4); L1STEP(5); L1STEP(6); L1STEP(7);
  } else {
    f32x4 qa[4][3];
#pragma unroll
    for (int t = 0; t < 4; ++t)
#pragma unroll
      for (int j = 0; j < 3; ++j) qa[t][j] = f32x4{0.f, 0.f, 0.f, 0.f};
    bf16x8 fa[2][4], fb[2][3];
    auto rda = [&](int d, int ks, int t) {
      const unsigned sb = lb + ROFF + (unsigned)(d % 3) * SLOT;
      bf16x4 h[2];
#pragma unroll
      for (int u = 0; u < 2; ++u) {
        const unsigned row = 32u * ks + 8u * g + 4u * u + q4;
        const unsigned ch = (8u * c + 2u * t + (p4 >> 1)) ^ (fsw(row) << 1);
        h[u] = lds_tr(sb + row * 512u + ch * 16u + 8u * (p4 & 1u));
      }
      fa[ks][t] = cat(h[0], h[1]);
    };
    auto rdb = [&](int d, int ks) {
      const unsigned kb = ((unsigned)d + rot) & 7u;
#pragma unroll
      for (int j = 0; j < 3; ++j) {
        const unsigned row = 16u * j + li;
        const unsigned ch = (8u * kb + 4u * ks + g) ^ li;
        fb[ks][j] = lds_b128(lb + XOFF + row * 1024u + ch * 16u);
      }
    };
    BARRIER(); STAMP(1);
#pragma unroll
    for (int ks = 0; ks < 2; ++ks) { rda(0, ks, 0); rdb(0, ks); rda(0, ks, 1); rda(0, ks, 2); rda(0, ks, 3); }
#define C1STEP(d) do { \
      BARRIER(); STAMP(2 + (d)); \
      _Pragma("unroll") for (int q = 0; q < 8; ++q) { \
        _Pragma("unroll") for (int j = 0; j < 3; ++j) qa[q & 3][j] = mfma(fa[q >> 2][q & 3], fb[q >> 2][j], qa[q & 3][j]); \
        SB(); \
        if ((d) + 1 < 8) { rda((d) + 1, q >> 2, q & 3); if ((q & 3) == 3) rdb((d) + 1, q >> 2); } \
        SB(); \
      } \
    } while (0)
    C1STEP(0); C1STEP(1); C1STEP(2); C1STEP(3); C1STEP(4); C1STEP(5); C1STEP(6); C1STEP(7);
#pragma unroll
    for (int t = 0; t < 4; ++t)
#pragma unroll
      for (int j = 0; j < 3; ++j) {
        const unsigned row = (c >> 1) * 48u + 16u * j + li;
        const unsigned d = 64u * (c & 1u) + 16u * t + 4u * g;
        u32x2 v = {cvtpk(qa[t][j][0], qa[t][j][1]), cvtpk(qa[t][j][2], qa[t][j][3])};
        *(LAS u32x2*)(uintptr_t)(lb + XOFF + row * 256u + (((d >> 3) ^ (row & 15u)) * 16u) + (d & 7u) * 2u) = v;
      }
  }

  const float CS = 0.088388347648318447f * 1.4426950408889634f;
  LAS float* smax = (LAS float*)(uintptr_t)(lb + SMAXOFF);
  LAS float* ssum = (LAS float*)(uintptr_t)(lb + SSUMOFF);
  bf16x8 qf[3][4], ka[2][4], vf[4][2], pf[4][3], wf[4][2];
  f32x4 sc[2][3], oa[2][3], lacc[3];
  float mrun[3], alpha[3];
#pragma unroll
  for (int j = 0; j < 3; ++j) {
    lacc[j] = f32x4{0.f, 0.f, 0.f, 0.f};
    oa[0][j] = f32x4{0.f, 0.f, 0.f, 0.f};
    oa[1][j] = f32x4{0.f, 0.f, 0.f, 0.f};
    mrun[j] = 0.f; alpha[j] = 1.f;
  }
  const bf16x8 ones = {0x3f80, 0x3f80, 0x3f80, 0x3f80, 0x3f80, 0x3f80, 0x3f80, 0x3f80};
  auto rdk = [&](int t, int kt, int s) {
    ka[kt][s] = lds_b128(lb + ROFF + (unsigned)(t % 3) * SLOT + (32u * c + 16u * kt + li) * 256u + (((4u * s + g) ^ li) * 16u));
  };
  auto rdv = [&](int t, int ks) {
    const unsigned row0 = 4u * g + q4;
    const unsigned a0 = lb + ROFF + (unsigned)(t % 3) * SLOT + row0 * 256u + ((((row0 & 7u) ^ (2u * c)) << 5)) + (p4 << 3);
#pragma unroll
    for (int tt = 0; tt < 2; ++tt) vf[ks][tt] = cat(lds_tr((a0 ^ (unsigned)(tt << 5)) + 8192u * ks), lds_tr((a0 ^ (unsigned)(tt << 5)) + 8192u * ks + 4096u));
  };
  auto rdp = [&](int js, int ks) {
    const unsigned pb = lb + XOFF + (unsigned)(js & 1) * PBUF + r * 12288u + lane * 16u;
#pragma unroll
    for (int j = 0; j < 3; ++j) pf[ks][j] = lds_b128(pb + (unsigned)(ks * 3 + j) * 1024u);
  };
  auto rdw = [&](int e, int ks) {
    const unsigned sb = lb + ROFF + (unsigned)((16 + e) % 3) * SLOT;
#pragma unroll
    for (int t = 0; t < 2; ++t) {
      bf16x4 h[2];
#pragma unroll
      for (int u = 0; u < 2; ++u) {
        const unsigned row = 32u * ks + 8u * g + 4u * u + q4;
        const unsigned ch = (4u * c + 2u * t + (p4 >> 1)) ^ (fsw(row) << 1);
        h[u] = lds_tr(sb + row * 256u + ch * 16u + 8u * (p4 & 1u));
      }
      wf[ks][t] = cat(h[0], h[1]);
    }
  };
  auto pvk = [&](int ks) {
#pragma unroll
    for (int t = 0; t < 2; ++t)
#pragma unroll
      for (int j = 0; j < 3; ++j) oa[t][j] = mfma(vf[ks][t], pf[ks][j], oa[t][j]);
  };
  auto rescale = [&]() {
#pragma unroll
    for (int t = 0; t < 2; ++t)
#pragma unroll
      for (int j = 0; j < 3; ++j) oa[t][j] *= alpha[j];
  };
  auto expo1 = [&](int js, int j) {
    const float nm = -mrun[j] * CS;
    float pe[8];
#pragma unroll
    for (int e = 0; e < 8; ++e) pe[e] = __builtin_amdgcn_exp2f(fmaf(sc[e >> 2][j][e & 3], CS, nm));
    const u32x4 pk = {cvtpk(pe[0], pe[1]), cvtpk(pe[2], pe[3]), cvtpk(pe[4], pe[5]), cvtpk(pe[6], pe[7])};
    if (js > 0) lacc[j][0] *= alpha[j];
    lacc[j] = mfma(ones, __builtin_bit_cast(bf16x8, pk), lacc[j]);
    *(LAS u32x4*)(uintptr_t)(lb + XOFF + (unsigned)(js & 1) * PBUF + (w * 3u + j) * 1024u + lane * 16u) = pk;
  };
#define OPEN(n) do { if ((n) == 0) waitv<16>(); else if ((n) == 1) waitv<8>(); else waitv<4>(); BARRIER(); STAMP(10 + (n)); } while (0)
#define AINT(js) do { \
    OPEN(2 * (js)); \
    if ((js) == 0) { \
      _Pragma("unroll") for (int s = 0; s < 4; ++s) { rdk(8, 0, s); \
        _Pragma("unroll") for (int j = 0; j < 3; ++j) { const unsigned row = r * 48u + 16u * j + li; \
          qf[j][s] = lds_b128(lb + XOFF + row * 256u + (((4u * s + g) ^ (row & 15u)) * 16u)); } } \
      _Pragma("unroll") for (int s = 0; s < 4; ++s) rdk(8, 1, s); \
      SB(); } \
    _Pragma("unroll") for (int kt = 0; kt < 2; ++kt) { \
      _Pragma("unroll") for (int j = 0; j < 3; ++j) sc[kt][j] = f32x4{0.f, 0.f, 0.f, 0.f}; \
      _Pragma("unroll") for (int s = 0; s < 4; ++s) { \
        _Pragma("unroll") for (int j = 0; j < 3; ++j) sc[kt][j] = mfma(ka[kt][s], qf[j][s], sc[kt][j]); \
        SB(); \
        if ((js) >= 1) { if (kt == 0) { rdv(8 + 2 * (js), s); piece(10 + 2 * (js), w * 4u, s); } else rdp((js) - 1, s); } \
        if ((js) >= 2 && s < 3) oa[kt][s] *= alpha[s]; \
        SB(); \
      } } \
    _Pragma("unroll") for (int j = 0; j < 3; ++j) { \
      const float m = max2f(max3f(sc[0][j][0], sc[0][j][1], sc[0][j][2]), max3f(max3f(sc[0][j][3], sc[1][j][0], sc[1][j][1]), sc[1][j][2], sc[1][j][3])); \
      lds_fmax(lb + SMAXOFF + (r * 48u + 16u * j + li) * 4u, m); } \
  } while (0)
#define BINT(js) do { \
    OPEN(2 * (js) + 1); \
    _Pragma("unroll") for (int j = 0; j < 3; ++j) { \
      const float m = smax[r * 48u + 16u * j + li]; \
      if ((js) > 0) alpha[j] = __builtin_amdgcn_exp2f((mrun[j] - m) * CS); \
      mrun[j] = m; } \
    SB(); if ((js) == 2) { FSTAMP(33); } \
    _Pragma("unroll") for (int ks = 0; ks < 4; ++ks) { \
      if ((js) >= 1) pvk(ks); \
      if (ks < 3) expo1(js, ks); \
      if ((js) >= 1 && ks < 3) { _Pragma("unroll") for (int z = 0; z < 6; ++z) { __builtin_amdgcn_sched_group_barrier(0x8, 1, 0); __builtin_amdgcn_sched_group_barrier(0x402, 4, 0); } } \
      SB(); if ((js) == 2) { FSTAMP(34 + 3 * ks); } \
      if ((js) < 3) { rdk(9 + 2 * (js), 0, ks); rdk(9 + 2 * (js), 1, ks); } else rdv(15, ks); \
      piece(11 + 2 * (js), w * 4u, ks); \
      SB(); if ((js) == 2) { FSTAMP(35 + 3 * ks); } \
    } \
  } while (0)
  AINT(0); BINT(0); AINT(1); BINT(1); AINT(2); BINT(2); AINT(3); BINT(3);
#pragma unroll
  for (int j = 0; j < 3; ++j) { ssum[(r * 4u + c) * 48u + 16u * j + li] = lacc[j][0]; }
  OPEN(8);
#pragma unroll
  for (int ks = 0; ks < 4; ++ks) rdp(3, ks);
  rescale();
  float inv[3];
#pragma unroll
  for (int j = 0; j < 3; ++j)
    inv[j] = __builtin_amdgcn_rcpf((ssum[(r * 4u + 0u) * 48u + 16u * j + li] + ssum[(r * 4u + 1u) * 48u + 16u * j + li]) +
                                   (ssum[(r * 4u + 2u) * 48u + 16u * j + li] + ssum[(r * 4u + 3u) * 48u + 16u * j + li]));
#pragma unroll
  for (int ks = 0; ks < 4; ++ks) {
    pvk(ks);
    SB();
    piece(18, w * 4u, ks);
    if (!loader) rdw(0, ks);
    SB();
  }
  STAMP(19);
#pragma unroll
  for (int j = 0; j < 3; ++j)
#pragma unroll
    for (int t = 0; t < 2; ++t) {
      const unsigned row = 16u * j + li;
      const unsigned k = r * 128u + 32u * c + 16u * t + 4u * g;
      u32x2 v = {cvtpk(oa[t][j][0] * inv[j], oa[t][j][1] * inv[j]), cvtpk(oa[t][j][2] * inv[j], oa[t][j][3] * inv[j])};
      *(LAS u32x2*)(uintptr_t)(lb + XOFF + row * 512u + (((k >> 3) ^ li) * 16u) + (k & 7u) * 2u) = v;
    }
  STAMP(20); STAMP(21); STAMP(22);

  if (loader) {
#define L3STEP(e) do { if ((e) == 0) waitv<4>(); else if ((e) < 6) waitv<8>(); else waitv<0>(); BARRIER(); STAMP(23 + (e)); if ((e) + 3 < 8) issue8(19 + (e)); } while (0)
    L3STEP(0); L3STEP(1); L3STEP(2); L3STEP(3); L3STEP(4); L3STEP(5); L3STEP(6);
  } else {
    bf16x8 of[3][8];
    f32x4 pa[2][3];
#define C3STEP(e) do { \
      if ((e) < 7) { if ((e) == 0) waitv<4>(); else if ((e) == 1) waitv<0>(); BARRIER(); } STAMP(23 + (e)); \
      if ((e) == 0) { \
        _Pragma("unroll") for (int s = 0; s < 8; ++s) \
        _Pragma("unroll") for (int j = 0; j < 3; ++j) of[j][s] = lds_b128(lb + XOFF + (16u * j + li) * 512u + (((4u * s + g) ^ li) * 16u)); } \
      if (!((e) & 1)) { _Pragma("unroll") for (int j = 0; j < 3; ++j) { pa[0][j] = f32x4{0.f, 0.f, 0.f, 0.f}; pa[1][j] = f32x4{0.f, 0.f, 0.f, 0.f}; } } \
      SB(); \
      _Pragma("unroll") for (int ks = 0; ks < 4; ++ks) { \
        _Pragma("unroll") for (int t = 0; t < 2; ++t) \
        _Pragma("unroll") for (int j = 0; j < 3; ++j) pa[t][j] = mfma(wf[ks][t], of[j][4 * ((e) & 1) + ks], pa[t][j]); \
        SB(); if ((e) + 1 < 8) rdw((e) + 1, ks); SB(); \
      } \
      if ((e) & 1) { const unsigned nb = (((unsigned)(e) >> 1) + nrot) & 3u; \
        _Pragma("unroll") for (int t = 0; t < 2; ++t) \
        _Pragma("unroll") for (int j = 0; j < 3; ++j) \
          __builtin_amdgcn_raw_buffer_store_b64(u32x2{cvtpk(pa[t][j][0], pa[t][j][1]), cvtpk(pa[t][j][2], pa[t][j][3])}, rp, lane * 8u + 512u * j + 1536u * t, (nb * 8u + 2u * c) * 1536u, 0); } \
    } while (0)
    C3STEP(0); C3STEP(1); C3STEP(2); C3STEP(3); C3STEP(4); C3STEP(5); C3STEP(6); C3STEP(7);
  }
}

__global__ __launch_bounds__(256)
void gqa_reduce(const unsigned short* __restrict__ part, unsigned short* __restrict__ out) {
  const unsigned i = blockIdx.x * 256u + threadIdx.x;
  const unsigned b = i / 3072u, e = i % 3072u;
  const u32x4* p = (const u32x4*)part + (size_t)b * 16u * 3072u + e;
  u32x4 v[16];
#pragma unroll
  for (int h = 0; h < 16; ++h) v[h] = p[(size_t)h * 3072u];
  float s[8];
#pragma unroll
  for (int k = 0; k < 8; ++k) s[k] = 0.f;
#pragma unroll
  for (int h = 0; h < 16; ++h)
#pragma unroll
    for (int k = 0; k < 4; ++k) {
      s[2 * k] += __uint_as_float(v[h][k] << 16);
      s[2 * k + 1] += __uint_as_float(v[h][k] & 0xffff0000u);
    }
  const unsigned l2 = e & 31u, j = (e >> 5) % 3u, nw = e / 96u;
  const unsigned lane = 2u * l2, g = lane >> 4, li = lane & 15u;
  unsigned short* o = out + ((size_t)b * SQ + 16u * j + li) * DM + 16u * nw + 4u * g;
  *(u32x2*)o = u32x2{cvtpk(s[0], s[1]), cvtpk(s[2], s[3])};
  *(u32x2*)(o + DM) = u32x2{cvtpk(s[4], s[5]), cvtpk(s[6], s[7])};
}

extern "C" void kernel_launch(void* const* d_in, const int* in_sizes, int n_in,
                              void* d_out, int out_size, void* d_ws, size_t ws_size,
                              hipStream_t stream) {
  (void)in_sizes; (void)n_in; (void)out_size; (void)ws_size;
  (void)hipFuncSetAttribute((const void*)gqa_fused, hipFuncAttributeMaxDynamicSharedMemorySize, LDS_BYTES);
  const unsigned short* x = (const unsigned short*)d_in[0];
  const unsigned short* kc = (const unsigned short*)d_in[1];
  const unsigned short* vc = (const unsigned short*)d_in[2];
  const unsigned short* wq = (const unsigned short*)d_in[3];
  const unsigned short* wo = (const unsigned short*)d_in[4];
  unsigned short* part = (unsigned short*)d_ws;
  hipLaunchKernelGGL(gqa_fused, dim3(256), dim3(512), LDS_BYTES, stream, x, kc, vc, wq, wo, part);
  hipLaunchKernelGGL(gqa_reduce, dim3(NB * 3072 / 256), dim3(256), 0, stream, (const unsigned short*)part, (unsigned short*)d_out);
}
```

```cpp
#include <hip/hip_runtime.h>
#include <hip/hip_bf16.h>
#include <stdint.h>

typedef short bf16x8 __attribute__((ext_vector_type(8)));
typedef short bf16x4 __attribute__((ext_vector_type(4)));
typedef float f32x4 __attribute__((ext_vector_type(4)));
typedef float f32x2 __attribute__((ext_vector_type(2)));
typedef __bf16 bf2 __attribute__((ext_vector_type(2)));
typedef unsigned u32x2 __attribute__((ext_vector_type(2)));
typedef unsigned u32x4 __attribute__((ext_vector_type(4)));
#define LAS __attribute__((address_space(3)))
#define DI __device__ __forceinline__

constexpr int NB = 16, SQ = 48, DM = 512, HQ = 32, HKV = 8, SK = 512, DH = 128;
constexpr unsigned XOFF = 0;
constexpr unsigned ROFF = 65536;
constexpr unsigned SLOT = 32768;
constexpr unsigned PBUF = 24576;
constexpr unsigned SMAXOFF = 49152, SSUMOFF = 51200;
constexpr unsigned LDS_BYTES = 163840;
#define POLLSLEEP 4
constexpr size_t FLAG_OFF = 16u << 20, EPOCH_OFF = 17u << 20;
typedef __attribute__((address_space(1))) unsigned long long gu64;

DI unsigned rfl(unsigned x) { return __builtin_amdgcn_readfirstlane(x); }
DI unsigned cvtpk(float lo, float hi) {
  f32x2 v = {lo, hi};
  bf2 b = __builtin_convertvector(v, bf2);
  return __builtin_bit_cast(unsigned, b);
}
#define BARRIER() asm volatile("s_waitcnt lgkmcnt(0)\n\ts_barrier" ::: "memory")
#define SB() __builtin_amdgcn_sched_barrier(0)
template <int N> DI void waitv() { asm volatile("s_waitcnt vmcnt(%0)" :: "i"(N > 63 ? 63 : N) : "memory"); }
#define STAMP(k)
#define FSTAMP(k)
#define STAMPRT(k)

template <int NT> DI void dma(__amdgpu_buffer_rsrc_t rsrc, unsigned voff, unsigned soff, unsigned ldsdst) {
  if (NT) asm volatile("s_mov_b32 m0, %0\n\ts_nop 0\n\tbuffer_load_dwordx4 %1, %2, %3 offen nt lds"
               :: "s"(ldsdst), "v"(voff), "s"(rsrc), "s"(soff) : "memory");
  else asm volatile("s_mov_b32 m0, %0\n\ts_nop 0\n\tbuffer_load_dwordx4 %1, %2, %3 offen lds"
               :: "s"(ldsdst), "v"(voff), "s"(rsrc), "s"(soff) : "memory");
}
DI bf16x8 lds_b128(unsigned a) { return *(const LAS bf16x8*)(uintptr_t)a; }
DI bf16x4 lds_tr(unsigned a) { return __builtin_amdgcn_ds_read_tr16_b64_v4i16((LAS bf16x4*)(uintptr_t)a); }
DI bf16x8 cat(bf16x4 a, bf16x4 b) { return __builtin_shufflevector(a, b, 0, 1, 2, 3, 4, 5, 6, 7); }
DI f32x4 mfma(bf16x8 a, bf16x8 b, f32x4 c) { return __builtin_amdgcn_mfma_f32_16x16x32_bf16(a, b, c, 0, 0, 0); }
DI unsigned fsw(unsigned row) { return (row & 3u) | (((row >> 3) & 1u) << 2); }
DI float max2f(float a, float b) { return __builtin_elementwise_maximum(a, b); }
DI float max3f(float a, float b, float c) { return max2f(max2f(a, b), c); }
DI void lds_fmax(unsigned a, float v) { asm volatile("ds_max_f32 %0, %1" :: "v"(a), "v"(v) : "memory"); }
DI float xmax4(float v) {
  auto a = __builtin_amdgcn_permlane32_swap(__float_as_uint(v), __float_as_uint(v), false, false);
  v = fmaxf(__uint_as_float(a[0]), __uint_as_float(a[1]));
  auto b = __builtin_amdgcn_permlane16_swap(__float_as_uint(v), __float_as_uint(v), false, false);
  return fmaxf(__uint_as_float(b[0]), __uint_as_float(b[1]));
}

__global__ __launch_bounds__(512, 2)
void gqa_fused(const unsigned short* __restrict__ x, const unsigned short* __restrict__ kc,
               const unsigned short* __restrict__ vc, const unsigned short* __restrict__ wq,
               const unsigned short* __restrict__ wo, unsigned short* __restrict__ part,
               unsigned short* __restrict__ out) {
  extern __shared__ __attribute__((aligned(16))) char smem[];
  const unsigned lb = (unsigned)(uintptr_t)(LAS char*)smem;
  const unsigned tid = threadIdx.x, lane = tid & 63u;
  const unsigned w = rfl(tid >> 6);
  const unsigned bid = blockIdx.x, hkv = bid & 7u, idx = bid >> 3, b = idx >> 1, half = idx & 1u;
  const unsigned h0 = hkv * 4u + half * 2u;
  const unsigned g = lane >> 4, li = lane & 15u, q4 = li >> 2, p4 = lane & 3u;
  const unsigned r = w >> 2, c = w & 3u;
  const unsigned rot = b & 7u, nrot = b & 3u;
  const bool loader = (r == 0);

  const __amdgpu_buffer_rsrc_t rx = __builtin_amdgcn_make_buffer_rsrc((void*)(x + (size_t)b * SQ * DM), (short)0, SQ * DM * 2, 0x00020000);
  const __amdgpu_buffer_rsrc_t rwq = __builtin_amdgcn_make_buffer_rsrc((void*)wq, (short)0, DM * HQ * DH * 2, 0x00020000);
  const __amdgpu_buffer_rsrc_t rwo = __builtin_amdgcn_make_buffer_rsrc((void*)wo, (short)0, DM * HQ * DH * 2, 0x00020000);
  const __amdgpu_buffer_rsrc_t rk = __builtin_amdgcn_make_buffer_rsrc((void*)(kc + ((size_t)b * HKV + hkv) * SK * DH), (short)0, SK * DH * 2, 0x00020000);
  const __amdgpu_buffer_rsrc_t rv = __builtin_amdgcn_make_buffer_rsrc((void*)(vc + ((size_t)b * HKV + hkv) * SK * DH), (short)0, SK * DH * 2, 0x00020000);
  const unsigned hp = hkv * 2u + half;
  const __amdgpu_buffer_rsrc_t rp = __builtin_amdgcn_make_buffer_rsrc((void*)(part + (size_t)b * 16u * (SQ * DM)), (short)0, 16 * SQ * DM * 2, 0x00020000);
  gu64* flags = (gu64*)((char*)part + FLAG_OFF) + (size_t)b * 256u;
  gu64* epoch = (gu64*)((char*)part + EPOCH_OFF) + (size_t)b * 16u;
  unsigned long long tag;
  auto gettag = [&]() {
    unsigned long long e;
    asm volatile("s_load_dwordx2 %0, %1, 0x0\n\ts_waitcnt lgkmcnt(0)" : "=&s"(e) : "s"(epoch) : "memory");
    tag = e * 0x9E3779B97F4A7C15ull + 0xD1B54A32D192ED03ull;
  };

  auto piece = [&](int t, unsigned pb, int i) {
    const unsigned dst = lb + ROFF + (unsigned)(t % 3) * SLOT + pb * 1024u + 1024u * i;
    if (t < 8) {
      const unsigned kb = ((unsigned)t + rot) & 7u;
      const unsigned row = 2u * i + (lane >> 5);
      const unsigned ch = (lane & 31u) ^ (fsw(row) << 1);
      dma<0>(rwq, row * 8192u + ch * 16u, (kb * 64u + pb * 2u) * 8192u + h0 * 256u, dst);
    } else if (t < 16) {
      const bool isk = (t == 8) || (t & 1 && t < 15);
      const int blk = t == 8 ? 0 : (t == 15 ? 3 : (isk ? (t - 7) / 2 : (t - 10) / 2));
      const unsigned row = 4u * i + g;
      const unsigned ch = isk ? (li ^ (row & 15u)) : (li ^ ((row & 7u) << 1));
      dma<1>(isk ? rk : rv, row * 256u + ch * 16u, (unsigned)blk * 32768u + pb * 1024u, dst);
    } else {
      const unsigned nb = (((unsigned)(t - 16) >> 1) + nrot) & 3u, kh = (unsigned)(t - 16) & 1u;
      const unsigned row = 4u * i + g;
      const unsigned ch = li ^ (fsw(row) << 1);
      dma<0>(rwo, row * 1024u + ch * 16u, (h0 * 128u + kh * 128u + pb * 4u) * 1024u + nb * 256u, dst);
    }
  };
  auto issue8 = [&](int t) {
#pragma unroll
    for (int i = 0; i < 8; ++i) piece(t, c * 8u, i);
  };

  STAMPRT(NSTAMP - 2);
  STAMP(0);
  if (loader) {
#pragma unroll
    for (int i = 0; i < 12; ++i) {
      const unsigned row = c * 12u + i;
      const unsigned ch = lane ^ (row & 15u);
      dma<0>(rx, ch * 16u, row * 1024u, lb + XOFF + row * 1024u);
    }
    issue8(0); issue8(1); issue8(2);
    if (tid < 96u) *(LAS float*)(uintptr_t)(lb + SMAXOFF + tid * 4u) = -__builtin_inff();
    gettag();
    waitv<16>();
    BARRIER(); STAMP(1);
#define L1STEP(d) do { if ((d) < 7) waitv<8>(); BARRIER(); STAMP(2 + (d)); issue8((d) + 3); } while (0)
    L1STEP(0); L1STEP(1); L1STEP(2); L1STEP(3); L1STEP(4); L1STEP(5); L1STEP(6); L1STEP(7);
  } else {
    gettag();
    f32x4 qa[4][3];
#pragma unroll
    for (int t = 0; t < 4; ++t)
#pragma unroll
      for (int j = 0; j < 3; ++j) qa[t][j] = f32x4{0.f, 0.f, 0.f, 0.f};
    bf16x8 fa[2][4], fb[2][3];
    auto rda = [&](int d, int ks, int t) {
      const unsigned sb = lb + ROFF + (unsigned)(d % 3) * SLOT;
      bf16x4 h[2];
#pragma unroll
      for (int u = 0; u < 2; ++u) {
        const unsigned row = 32u * ks + 8u * g + 4u * u + q4;
        const unsigned ch = (8u * c + 2u * t + (p4 >> 1)) ^ (fsw(row) << 1);
        h[u] = lds_tr(sb + row * 512u + ch * 16u + 8u * (p4 & 1u));
      }
      fa[ks][t] = cat(h[0], h[1]);
    };
    auto rdb = [&](int d, int ks) {
      const unsigned kb = ((unsigned)d + rot) & 7u;
#pragma unroll
      for (int j = 0; j < 3; ++j) {
        const unsigned row = 16u * j + li;
        const unsigned ch = (8u * kb + 4u * ks + g) ^ li;
        fb[ks][j] = lds_b128(lb + XOFF + row * 1024u + ch * 16u);
      }
    };
    BARRIER(); STAMP(1);
#pragma unroll
    for (int ks = 0; ks < 2; ++ks) { rda(0, ks, 0); rdb(0, ks); rda(0, ks, 1); rda(0, ks, 2); rda(0, ks, 3); }
#define C1STEP(d) do { \
      BARRIER(); STAMP(2 + (d)); \
      _Pragma("unroll") for (int q = 0; q < 8; ++q) { \
        _Pragma("unroll") for (int j = 0; j < 3; ++j) qa[q & 3][j] = mfma(fa[q >> 2][q & 3], fb[q >> 2][j], qa[q & 3][j]); \
        SB(); \
        if ((d) + 1 < 8) { rda((d) + 1, q >> 2, q & 3); if ((q & 3) == 3) rdb((d) + 1, q >> 2); } \
        SB(); \
      } \
    } while (0)
    C1STEP(0); C1STEP(1); C1STEP(2); C1STEP(3); C1STEP(4); C1STEP(5); C1STEP(6); C1STEP(7);
#pragma unroll
    for (int t = 0; t < 4; ++t)
#pragma unroll
      for (int j = 0; j < 3; ++j) {
        const unsigned row = (c >> 1) * 48u + 16u * j + li;
        const unsigned d = 64u * (c & 1u) + 16u * t + 4u * g;
        u32x2 v = {cvtpk(qa[t][j][0], qa[t][j][1]), cvtpk(qa[t][j][2], qa[t][j][3])};
        *(LAS u32x2*)(uintptr_t)(lb + XOFF + row * 256u + (((d >> 3) ^ (row & 15u)) * 16u) + (d & 7u) * 2u) = v;
      }
  }

  const float CS = 0.088388347648318447f * 1.4426950408889634f;
  LAS float* smax = (LAS float*)(uintptr_t)(lb + SMAXOFF);
  LAS float* ssum = (LAS float*)(uintptr_t)(lb + SSUMOFF);
  bf16x8 qf[3][4], ka[2][4], vf[4][2], pf[4][3], wf[4][2];
  f32x4 sc[2][3], oa[2][3], lacc[3];
  float mrun[3], alpha[3];
#pragma unroll
  for (int j = 0; j < 3; ++j) {
    lacc[j] = f32x4{0.f, 0.f, 0.f, 0.f};
    oa[0][j] = f32x4{0.f, 0.f, 0.f, 0.f};
    oa[1][j] = f32x4{0.f, 0.f, 0.f, 0.f};
    mrun[j] = 0.f; alpha[j] = 1.f;
  }
  const bf16x8 ones = {0x3f80, 0x3f80, 0x3f80, 0x3f80, 0x3f80, 0x3f80, 0x3f80, 0x3f80};
  auto rdk = [&](int t, int kt, int s) {
    ka[kt][s] = lds_b128(lb + ROFF + (unsigned)(t % 3) * SLOT + (32u * c + 16u * kt + li) * 256u + (((4u * s + g) ^ li) * 16u));
  };
  auto rdv = [&](int t, int ks) {
    const unsigned row0 = 4u * g + q4;
    const unsigned a0 = lb + ROFF + (unsigned)(t % 3) * SLOT + row0 * 256u + ((((row0 & 7u) ^ (2u * c)) << 5)) + (p4 << 3);
#pragma unroll
    for (int tt = 0; tt < 2; ++tt) vf[ks][tt] = cat(lds_tr((a0 ^ (unsigned)(tt << 5)) + 8192u * ks), lds_tr((a0 ^ (unsigned)(tt << 5)) + 8192u * ks + 4096u));
  };
  auto rdp = [&](int js, int ks) {
    const unsigned pb = lb + XOFF + (unsigned)(js & 1) * PBUF + r * 12288u + lane * 16u;
#pragma unroll
    for (int j = 0; j < 3; ++j) pf[ks][j] = lds_b128(pb + (unsigned)(ks * 3 + j) * 1024u);
  };
  auto rdw = [&](int e, int ks) {
    const unsigned sb = lb + ROFF + (unsigned)((16 + e) % 3) * SLOT;
#pragma unroll
    for (int t = 0; t < 2; ++t) {
      bf16x4 h[2];
#pragma unroll
      for (int u = 0; u < 2; ++u) {
        const unsigned row = 32u * ks + 8u * g + 4u * u + q4;
        const unsigned ch = (4u * c + 2u * t + (p4 >> 1)) ^ (fsw(row) << 1);
        h[u] = lds_tr(sb + row * 256u + ch * 16u + 8u * (p4 & 1u));
      }
      wf[ks][t] = cat(h[0], h[1]);
    }
  };
  auto pvk = [&](int ks) {
#pragma unroll
    for (int t = 0; t < 2; ++t)
#pragma unroll
      for (int j = 0; j < 3; ++j) oa[t][j] = mfma(vf[ks][t], pf[ks][j], oa[t][j]);
  };
  auto rescale = [&]() {
#pragma unroll
    for (int t = 0; t < 2; ++t)
#pragma unroll
      for (int j = 0; j < 3; ++j) oa[t][j] *= alpha[j];
  };
  auto expo1 = [&](int js, int j) {
    const float nm = -mrun[j] * CS;
    float pe[8];
#pragma unroll
    for (int e = 0; e < 8; ++e) pe[e] = __builtin_amdgcn_exp2f(fmaf(sc[e >> 2][j][e & 3], CS, nm));
    const u32x4 pk = {cvtpk(pe[0], pe[1]), cvtpk(pe[2], pe[3]), cvtpk(pe[4], pe[5]), cvtpk(pe[6], pe[7])};
    if (js > 0) lacc[j][0] *= alpha[j];
    lacc[j] = mfma(ones, __builtin_bit_cast(bf16x8, pk), lacc[j]);
    *(LAS u32x4*)(uintptr_t)(lb + XOFF + (unsigned)(js & 1) * PBUF + (w * 3u + j) * 1024u + lane * 16u) = pk;
  };
#define OPEN(n) do { if ((n) == 0) waitv<16>(); else if ((n) == 1) waitv<8>(); else waitv<4>(); BARRIER(); STAMP(10 + (n)); } while (0)
#define AINT(js) do { \
    OPEN(2 * (js)); \
    if ((js) == 0) { \
      _Pragma("unroll") for (int s = 0; s < 4; ++s) { rdk(8, 0, s); \
        _Pragma("unroll") for (int j = 0; j < 3; ++j) { const unsigned row = r * 48u + 16u * j + li; \
          qf[j][s] = lds_b128(lb + XOFF + row * 256u + (((4u * s + g) ^ (row & 15u)) * 16u)); } } \
      _Pragma("unroll") for (int s = 0; s < 4; ++s) rdk(8, 1, s); \
      SB(); } \
    _Pragma("unroll") for (int kt = 0; kt < 2; ++kt) { \
      _Pragma("unroll") for (int j = 0; j < 3; ++j) sc[kt][j] = f32x4{0.f, 0.f, 0.f, 0.f}; \
      _Pragma("unroll") for (int s = 0; s < 4; ++s) { \
        _Pragma("unroll") for (int j = 0; j < 3; ++j) sc[kt][j] = mfma(ka[kt][s], qf[j][s], sc[kt][j]); \
        SB(); \
        if ((js) >= 1) { if (kt == 0) { rdv(8 + 2 * (js), s); piece(10 + 2 * (js), w * 4u, s); } else rdp((js) - 1, s); } \
        if ((js) >= 2 && s < 3) oa[kt][s] *= alpha[s]; \
        SB(); \
      } } \
    _Pragma("unroll") for (int j = 0; j < 3; ++j) { \
      const float m = max2f(max3f(sc[0][j][0], sc[0][j][1], sc[0][j][2]), max3f(max3f(sc[0][j][3], sc[1][j][0], sc[1][j][1]), sc[1][j][2], sc[1][j][3])); \
      lds_fmax(lb + SMAXOFF + (r * 48u + 16u * j + li) * 4u, m); } \
  } while (0)
#define BINT(js) do { \
    OPEN(2 * (js) + 1); \
    _Pragma("unroll") for (int j = 0; j < 3; ++j) { \
      const float m = smax[r * 48u + 16u * j + li]; \
      if ((js) > 0) alpha[j] = __builtin_amdgcn_exp2f((mrun[j] - m) * CS); \
      mrun[j] = m; } \
    _Pragma("unroll") for (int ks = 0; ks < 4; ++ks) { \
      if ((js) >= 1) pvk(ks); \
      if (ks < 3) expo1(js, ks); \
      if ((js) >= 1 && ks < 3) { _Pragma("unroll") for (int z = 0; z < 6; ++z) { __builtin_amdgcn_sched_group_barrier(0x8, 1, 0); __builtin_amdgcn_sched_group_barrier(0x402, 4, 0); } } \
      SB(); \
      if ((js) < 3) { rdk(9 + 2 * (js), 0, ks); rdk(9 + 2 * (js), 1, ks); } else rdv(15, ks); \
      piece(11 + 2 * (js), w * 4u, ks); \
      SB(); \
    } \
  } while (0)
  AINT(0); BINT(0); AINT(1); BINT(1); AINT(2); BINT(2); AINT(3); BINT(3);
#pragma unroll
  for (int j = 0; j < 3; ++j) { ssum[(r * 4u + c) * 48u + 16u * j + li] = lacc[j][0]; }
  OPEN(8);
#pragma unroll
  for (int ks = 0; ks < 4; ++ks) rdp(3, ks);
  rescale();
  float inv[3];
#pragma unroll
  for (int j = 0; j < 3; ++j)
    inv[j] = __builtin_amdgcn_rcpf((ssum[(r * 4u + 0u) * 48u + 16u * j + li] + ssum[(r * 4u + 1u) * 48u + 16u * j + li]) +
                                   (ssum[(r * 4u + 2u) * 48u + 16u * j + li] + ssum[(r * 4u + 3u) * 48u + 16u * j + li]));
#pragma unroll
  for (int ks = 0; ks < 4; ++ks) {
    pvk(ks);
    SB();
    piece(18, w * 4u, ks);
    if (!loader) rdw(0, ks);
    SB();
  }
  STAMP(19);
#pragma unroll
  for (int j = 0; j < 3; ++j)
#pragma unroll
    for (int t = 0; t < 2; ++t) {
      const unsigned row = 16u * j + li;
      const unsigned k = r * 128u + 32u * c + 16u * t + 4u * g;
      u32x2 v = {cvtpk(oa[t][j][0] * inv[j], oa[t][j][1] * inv[j]), cvtpk(oa[t][j][2] * inv[j], oa[t][j][3] * inv[j])};
      *(LAS u32x2*)(uintptr_t)(lb + XOFF + row * 512u + (((k >> 3) ^ li) * 16u) + (k & 7u) * 2u) = v;
    }
  STAMP(20); STAMP(21); STAMP(22);

  if (loader) {
#define L3STEP(e) do { if ((e) == 0) waitv<4>(); else if ((e) < 6) waitv<8>(); else waitv<0>(); BARRIER(); STAMP(23 + (e)); if ((e) + 3 < 8) issue8(19 + (e)); } while (0)
    L3STEP(0); L3STEP(1); L3STEP(2); L3STEP(3); L3STEP(4); L3STEP(5); L3STEP(6);
    BARRIER();
    if (c == 0) {
      for (unsigned spin = 0; spin < (1u << 20); ++spin) {
        const unsigned long long f = __hip_atomic_load(flags + hp * 16u + li, __ATOMIC_RELAXED, __HIP_MEMORY_SCOPE_AGENT);
        if (__all(f == tag)) break;
        __builtin_amdgcn_s_sleep(POLLSLEEP);
      }
    }
    BARRIER();
    if (c < 3) {
      STAMP(30);
      u32x4 v[16];
#pragma unroll
      for (int p = 0; p < 16; ++p) v[p] = __builtin_amdgcn_raw_buffer_load_b128(rp, ((hp * 3u + c) * 64u + lane) * 16u, p * (SQ * DM * 2), 16);
      float sum[8];
#pragma unroll
      for (int k = 0; k < 8; ++k) sum[k] = 0.f;
#pragma unroll
      for (int p = 0; p < 16; ++p)
#pragma unroll
        for (int k = 0; k < 4; ++k) {
          sum[2 * k] += __uint_as_float(v[p][k] << 16);
          sum[2 * k + 1] += __uint_as_float(v[p][k] & 0xffff0000u);
        }
      unsigned short* o = out + ((size_t)b * SQ + 16u * c + li) * DM + 32u * hp + 4u * g;
      *(u32x2*)o = u32x2{cvtpk(sum[0], sum[1]), cvtpk(sum[2], sum[3])};
      *(u32x2*)(o + 16) = u32x2{cvtpk(sum[4], sum[5]), cvtpk(sum[6], sum[7])};
      if (hp == 0 && c == 0 && lane == 0) __hip_atomic_store(epoch, tag, __ATOMIC_RELAXED, __HIP_MEMORY_SCOPE_AGENT);
    }
  } else {
    bf16x8 of[3][8];
    f32x4 pa[2][3];
    auto setflag = [&](int blk) {
      const unsigned nb = ((unsigned)blk + nrot) & 3u;
      if (lane == 0) __hip_atomic_store(flags + (nb * 4u + c) * 16u + hp, tag, __ATOMIC_RELAXED, __HIP_MEMORY_SCOPE_AGENT);
    };
#define C3STEP(e) do { \
      if ((e) < 7) { if ((e) == 0) waitv<4>(); else if ((e) == 1) waitv<0>(); BARRIER(); } STAMP(23 + (e)); \
      if ((e) == 0) { \
        _Pragma("unroll") for (int s = 0; s < 8; ++s) \
        _Pragma("unroll") for (int j = 0; j < 3; ++j) of[j][s] = lds_b128(lb + XOFF + (16u * j + li) * 512u + (((4u * s + g) ^ li) * 16u)); } \
      if (!((e) & 1)) { _Pragma("unroll") for (int j = 0; j < 3; ++j) { pa[0][j] = f32x4{0.f, 0.f, 0.f, 0.f}; pa[1][j] = f32x4{0.f, 0.f, 0.f, 0.f}; } } \
      SB(); \
      _Pragma("unroll") for (int ks = 0; ks < 4; ++ks) { \
        _Pragma("unroll") for (int t = 0; t < 2; ++t) \
        _Pragma("unroll") for (int j = 0; j < 3; ++j) pa[t][j] = mfma(wf[ks][t], of[j][4 * ((e) & 1) + ks], pa[t][j]); \
        SB(); if ((e) + 1 < 8) rdw((e) + 1, ks); SB(); \
      } \
      if ((e) & 1) { const unsigned nb = (((unsigned)(e) >> 1) + nrot) & 3u; \
        _Pragma("unroll") for (int j = 0; j < 3; ++j) \
          __builtin_amdgcn_raw_buffer_store_b128(u32x4{cvtpk(pa[0][j][0], pa[0][j][1]), cvtpk(pa[0][j][2], pa[0][j][3]), cvtpk(pa[1][j][0], pa[1][j][1]), cvtpk(pa[1][j][2], pa[1][j][3])}, \
                                                 rp, lane * 16u + 1024u * j, hp * (SQ * DM * 2) + (nb * 4u + c) * 3072u, 16); \
        if ((e) == 5) { waitv<6>(); setflag(0); } \
        if ((e) == 7) { STAMP(33); waitv<7>(); setflag(1); STAMP(34); waitv<4>(); setflag(2); STAMP(35); waitv<2>(); STAMP(36); setflag(3); } } \
    } while (0)
    C3STEP(0); C3STEP(1); C3STEP(2); C3STEP(3); C3STEP(4); C3STEP(5); C3STEP(6); C3STEP(7);
    BARRIER(); BARRIER();
  }
}

extern "C" void kernel_launch(void* const* d_in, const int* in_sizes, int n_in,
                              void* d_out, int out_size, void* d_ws, size_t ws_size,
                              hipStream_t stream) {
  (void)in_sizes; (void)n_in; (void)out_size; (void)ws_size;
  (void)hipFuncSetAttribute((const void*)gqa_fused, hipFuncAttributeMaxDynamicSharedMemorySize, LDS_BYTES);
  const unsigned short* x = (const unsigned short*)d_in[0];
  const unsigned short* kc = (const unsigned short*)d_in[1];
  const unsigned short* vc = (const unsigned short*)d_in[2];
  const unsigned short* wq = (const unsigned short*)d_in[3];
  const unsigned short* wo = (const unsigned short*)d_in[4];
  unsigned short* part = (unsigned short*)d_ws;
  hipLaunchKernelGGL(gqa_fused, dim3(256), dim3(512), LDS_BYTES, stream, x, kc, vc, wq, wo, part, (unsigned short*)d_out);
}
```

```cpp
#include <hip/hip_runtime.h>
#include <hip/hip_bf16.h>
#include <stdint.h>

typedef short bf16x8 __attribute__((ext_vector_type(8)));
typedef short bf16x4 __attribute__((ext_vector_type(4)));
typedef float f32x4 __attribute__((ext_vector_type(4)));
typedef float f32x2 __attribute__((ext_vector_type(2)));
typedef __bf16 bf2 __attribute__((ext_vector_type(2)));
typedef unsigned u32x2 __attribute__((ext_vector_type(2)));
typedef unsigned u32x4 __attribute__((ext_vector_type(4)));
#define LAS __attribute__((address_space(3)))
#define DI __device__ __forceinline__

constexpr int NB = 16, SQ = 48, DM = 512, HQ = 32, HKV = 8, SK = 512, DH = 128;
constexpr unsigned XOFF = 0;
constexpr unsigned ROFF = 65536;
constexpr unsigned SLOT = 32768;
constexpr unsigned PBUF = 24576;
constexpr unsigned SMAXOFF = 49152, SSUMOFF = 51200;
constexpr unsigned LDS_BYTES = 163840;
#define POLLSLEEP 4
constexpr size_t FLAG_OFF = 16u << 20, EPOCH_OFF = 17u << 20;
typedef __attribute__((address_space(1))) unsigned long long gu64;

DI unsigned rfl(unsigned x) { return __builtin_amdgcn_readfirstlane(x); }
DI unsigned cvtpk(float lo, float hi) {
  f32x2 v = {lo, hi};
  bf2 b = __builtin_convertvector(v, bf2);
  return __builtin_bit_cast(unsigned, b);
}
#define BARRIER() asm volatile("s_waitcnt lgkmcnt(0)\n\ts_barrier" ::: "memory")
#define SB() __builtin_amdgcn_sched_barrier(0)
template <int N> DI void waitv() { asm volatile("s_waitcnt vmcnt(%0)" :: "i"(N > 63 ? 63 : N) : "memory"); }
#define STAMP(k)
#define FSTAMP(k)
#define STAMPRT(k)

template <int NT> DI void dma(__amdgpu_buffer_rsrc_t rsrc, unsigned voff, unsigned soff, unsigned ldsdst) {
  if (NT) asm volatile("s_mov_b32 m0, %0\n\ts_nop 0\n\tbuffer_load_dwordx4 %1, %2, %3 offen nt lds"
               :: "s"(ldsdst), "v"(voff), "s"(rsrc), "s"(soff) : "memory");
  else asm volatile("s_mov_b32 m0, %0\n\ts_nop 0\n\tbuffer_load_dwordx4 %1, %2, %3 offen lds"
               :: "s"(ldsdst), "v"(voff), "s"(rsrc), "s"(soff) : "memory");
}
DI bf16x8 lds_b128(unsigned a) { return *(const LAS bf16x8*)(uintptr_t)a; }
DI bf16x4 lds_tr(unsigned a) { return __builtin_amdgcn_ds_read_tr16_b64_v4i16((LAS bf16x4*)(uintptr_t)a); }
DI bf16x8 cat(bf16x4 a, bf16x4 b) { return __builtin_shufflevector(a, b, 0, 1, 2, 3, 4, 5, 6, 7); }
DI f32x4 mfma(bf16x8 a, bf16x8 b, f32x4 c) { return __builtin_amdgcn_mfma_f32_16x16x32_bf16(a, b, c, 0, 0, 0); }
DI unsigned fsw(unsigned row) { return (row & 3u) | (((row >> 3) & 1u) << 2); }
DI float max2f(float a, float b) { return __builtin_elementwise_maximum(a, b); }
DI float max3f(float a, float b, float c) { return max2f(max2f(a, b), c); }
DI void lds_fmax(unsigned a, float v) { asm volatile("ds_max_f32 %0, %1" :: "v"(a), "v"(v) : "memory"); }
DI float xmax4(float v) {
  auto a = __builtin_amdgcn_permlane32_swap(__float_as_uint(v), __float_as_uint(v), false, false);
  v = fmaxf(__uint_as_float(a[0]), __uint_as_float(a[1]));
  auto b = __builtin_amdgcn_permlane16_swap(__float_as_uint(v), __float_as_uint(v), false, false);
  return fmaxf(__uint_as_float(b[0]), __uint_as_float(b[1]));
}

__global__ __launch_bounds__(512, 2)
void gqa_fused(const unsigned short* __restrict__ x, const unsigned short* __restrict__ kc,
               const unsigned short* __restrict__ vc, const unsigned short* __restrict__ wq,
               const unsigned short* __restrict__ wo, unsigned short* __restrict__ part,
               unsigned short* __restrict__ out) {
  extern __shared__ __attribute__((aligned(16))) char smem[];
  const unsigned lb = (unsigned)(uintptr_t)(LAS char*)smem;
  const unsigned tid = threadIdx.x, lane = tid & 63u;
  const unsigned w = rfl(tid >> 6);
  const unsigned bid = blockIdx.x, hkv = bid & 7u, idx = bid >> 3, b = idx >> 1, half = idx & 1u;
  const unsigned h0 = hkv * 4u + half * 2u;
  const unsigned g = lane >> 4, li = lane & 15u, q4 = li >> 2, p4 = lane & 3u;
  const unsigned r = w >> 2, c = w & 3u;
  const unsigned rot = b & 7u, nrot = b & 3u;
  const bool loader = (r == 0);

  const __amdgpu_buffer_rsrc_t rx = __builtin_amdgcn_make_buffer_rsrc((void*)(x + (size_t)b * SQ * DM), (short)0, SQ * DM * 2, 0x00020000);
  const __amdgpu_buffer_rsrc_t rwq = __builtin_amdgcn_make_buffer_rsrc((void*)wq, (short)0, DM * HQ * DH * 2, 0x00020000);
  const __amdgpu_buffer_rsrc_t rwo = __builtin_amdgcn_make_buffer_rsrc((void*)wo, (short)0, DM * HQ * DH * 2, 0x00020000);
  const __amdgpu_buffer_rsrc_t rk = __builtin_amdgcn_make_buffer_rsrc((void*)(kc + ((size_t)b * HKV + hkv) * SK * DH), (short)0, SK * DH * 2, 0x00020000);
  const __amdgpu_buffer_rsrc_t rv = __builtin_amdgcn_make_buffer_rsrc((void*)(vc + ((size_t)b * HKV + hkv) * SK * DH), (short)0, SK * DH * 2, 0x00020000);
  const unsigned hp = hkv * 2u + half;
  const __amdgpu_buffer_rsrc_t rp = __builtin_amdgcn_make_buffer_rsrc((void*)(part + (size_t)b * 16u * (SQ * DM)), (short)0, 16 * SQ * DM * 2, 0x00020000);
  gu64* flags = (gu64*)((char*)part + FLAG_OFF) + (size_t)b * 256u;
  gu64* epoch = (gu64*)((char*)part + EPOCH_OFF) + (size_t)b * 16u;
  unsigned long long tag;
  auto gettag = [&]() {
    unsigned long long e;
    asm volatile("s_load_dwordx2 %0, %1, 0x0\n\ts_waitcnt lgkmcnt(0)" : "=&s"(e) : "s"(epoch) : "memory");
    tag = e * 0x9E3779B97F4A7C15ull + 0xD1B54A32D192ED03ull;
  };

  auto piece = [&](int t, unsigned pb, int i) {
    const unsigned dst = lb + ROFF + (unsigned)(t % 3) * SLOT + pb * 1024u + 1024u * i;
    if (t < 8) {
      const unsigned kb = ((unsigned)t + rot) & 7u;
      const unsigned row = 2u * i + (lane >> 5);
      const unsigned ch = (lane & 31u) ^ (fsw(row) << 1);
      dma<0>(rwq, row * 8192u + ch * 16u, (kb * 64u + pb * 2u) * 8192u + h0 * 256u, dst);
    } else if (t < 16) {
      const bool isk = (t == 8) || (t & 1 && t < 15);
      const int blk = t == 8 ? 0 : (t == 15 ? 3 : (isk ? (t - 7) / 2 : (t - 10) / 2));
      const unsigned row = 4u * i + g;
      const unsigned ch = isk ? (li ^ (row & 15u)) : (li ^ ((row & 7u) << 1));
      dma<1>(isk ? rk : rv, row * 256u + ch * 16u, (unsigned)blk * 32768u + pb * 1024u, dst);
    } else {
      const unsigned nb = (((unsigned)(t - 16) >> 1) + nrot) & 3u, kh = (unsigned)(t - 16) & 1u;
      const unsigned row = 4u * i + g;
      const unsigned ch = li ^ (fsw(row) << 1);
      dma<0>(rwo, row * 1024u + ch * 16u, (h0 * 128u + kh * 128u + pb * 4u) * 1024u + nb * 256u, dst);
    }
  };
  auto issue8 = [&](int t) {
#pragma unroll
    for (int i = 0; i < 8; ++i) piece(t, c * 8u, i);
  };

  STAMPRT(NSTAMP - 2);
  STAMP(0);
  if (loader) {
#pragma unroll
    for (int i = 0; i < 12; ++i) {
      const unsigned row = c * 12u + i;
      const unsigned ch = lane ^ (row & 15u);
      dma<0>(rx, ch * 16u, row * 1024u, lb + XOFF + row * 1024u);
    }
    issue8(0); issue8(1); issue8(2);
    if (tid < 96u) *(LAS float*)(uintptr_t)(lb + SMAXOFF + tid * 4u) = -__builtin_inff();
    gettag();
    waitv<16>();
    BARRIER(); STAMP(1);
#define L1STEP(d) do { if ((d) < 7) waitv<8>(); BARRIER(); STAMP(2 + (d)); issue8((d) + 3); } while (0)
    L1STEP(0); L1STEP(1); L1STEP(2); L1STEP(3); L1STEP(4); L1STEP(5); L1STEP(6); L1STEP(7);
  } else {
    gettag();
    f32x4 qa[4][3];
#pragma unroll
    for (int t = 0; t < 4; ++t)
#pragma unroll
      for (int j = 0; j < 3; ++j) qa[t][j] = f32x4{0.f, 0.f, 0.f, 0.f};
    bf16x8 fa[2][4], fb[2][3];
    auto rda = [&](int d, int ks, int t) {
      const unsigned sb = lb + ROFF + (unsigned)(d % 3) * SLOT;
      bf16x4 h[2];
#pragma unroll
      for (int u = 0; u < 2; ++u) {
        const unsigned row = 32u * ks + 8u * g + 4u * u + q4;
        const unsigned ch = (8u * c + 2u * t + (p4 >> 1)) ^ (fsw(row) << 1);
        h[u] = lds_tr(sb + row * 512u + ch * 16u + 8u * (p4 & 1u));
      }
      fa[ks][t] = cat(h[0], h[1]);
    };
    auto rdb = [&](int d, int ks) {
      const unsigned kb = ((unsigned)d + rot) & 7u;
#pragma unroll
      for (int j = 0; j < 3; ++j) {
        const unsigned row = 16u * j + li;
        const unsigned ch = (8u * kb + 4u * ks + g) ^ li;
        fb[ks][j] = lds_b128(lb + XOFF + row * 1024u + ch * 16u);
      }
    };
    BARRIER(); STAMP(1);
#pragma unroll
    for (int ks = 0; ks < 2; ++ks) { rda(0, ks, 0); rdb(0, ks); rda(0, ks, 1); rda(0, ks, 2); rda(0, ks, 3); }
#define C1STEP(d) do { \
      BARRIER(); STAMP(2 + (d)); \
      _Pragma("unroll") for (int q = 0; q < 8; ++q) { \
        _Pragma("unroll") for (int j = 0; j < 3; ++j) qa[q & 3][j] = mfma(fa[q >> 2][q & 3], fb[q >> 2][j], qa[q & 3][j]); \
        SB(); \
        if ((d) + 1 < 8) { rda((d) + 1, q >> 2, q & 3); if ((q & 3) == 3) rdb((d) + 1, q >> 2); } \
        SB(); \
      } \
    } while (0)
    C1STEP(0); C1STEP(1); C1STEP(2); C1STEP(3); C1STEP(4); C1STEP(5); C1STEP(6); C1STEP(7);
#pragma unroll
    for (int t = 0; t < 4; ++t)
#pragma unroll
      for (int j = 0; j < 3; ++j) {
        const unsigned row = (c >> 1) * 48u + 16u * j + li;
        const unsigned d = 64u * (c & 1u) + 16u * t + 4u * g;
        u32x2 v = {cvtpk(qa[t][j][0], qa[t][j][1]), cvtpk(qa[t][j][2], qa[t][j][3])};
        *(LAS u32x2*)(uintptr_t)(lb + XOFF + row * 256u + (((d >> 3) ^ (row & 15u)) * 16u) + (d & 7u) * 2u) = v;
      }
  }

  const float CS = 0.088388347648318447f * 1.4426950408889634f;
  LAS float* smax = (LAS float*)(uintptr_t)(lb + SMAXOFF);
  LAS float* ssum = (LAS float*)(uintptr_t)(lb + SSUMOFF);
  bf16x8 qf[3][4], ka[2][4], vf[4][2], pf[4][3], wf[4][2];
  f32x4 sc[2][3], oa[2][3], lacc[3];
  float mrun[3], alpha[3];
#pragma unroll
  for (int j = 0; j < 3; ++j) {
    lacc[j] = f32x4{0.f, 0.f, 0.f, 0.f};
    oa[0][j] = f32x4{0.f, 0.f, 0.f, 0.f};
    oa[1][j] = f32x4{0.f, 0.f, 0.f, 0.f};
    mrun[j] = 0.f; alpha[j] = 1.f;
  }
  const bf16x8 ones = {0x3f80, 0x3f80, 0x3f80, 0x3f80, 0x3f80, 0x3f80, 0x3f80, 0x3f80};
  auto rdk = [&](int t, int kt, int s) {
    ka[kt][s] = lds_b128(lb + ROFF + (unsigned)(t % 3) * SLOT + (32u * c + 16u * kt + li) * 256u + (((4u * s + g) ^ li) * 16u));
  };
  auto rdv = [&](int t, int ks) {
    const unsigned row0 = 4u * g + q4;
    const unsigned a0 = lb + ROFF + (unsigned)(t % 3) * SLOT + row0 * 256u + ((((row0 & 7u) ^ (2u * c)) << 5)) + (p4 << 3);
#pragma unroll
    for (int tt = 0; tt < 2; ++tt) vf[ks][tt] = cat(lds_tr((a0 ^ (unsigned)(tt << 5)) + 8192u * ks), lds_tr((a0 ^ (unsigned)(tt << 5)) + 8192u * ks + 4096u));
  };
  auto rdp = [&](int js, int ks) {
    const unsigned pb = lb + XOFF + (unsigned)(js & 1) * PBUF + r * 12288u + lane * 16u;
#pragma unroll
    for (int j = 0; j < 3; ++j) pf[ks][j] = lds_b128(pb + (unsigned)(ks * 3 + j) * 1024u);
  };
  auto rdw = [&](int e, int ks) {
    const unsigned sb = lb + ROFF + (unsigned)((16 + e) % 3) * SLOT;
#pragma unroll
    for (int t = 0; t < 2; ++t) {
      bf16x4 h[2];
#pragma unroll
      for (int u = 0; u < 2; ++u) {
        const unsigned row = 32u * ks + 8u * g + 4u * u + q4;
        const unsigned ch = (4u * c + 2u * t + (p4 >> 1)) ^ (fsw(row) << 1);
        h[u] = lds_tr(sb + row * 256u + ch * 16u + 8u * (p4 & 1u));
      }
      wf[ks][t] = cat(h[0], h[1]);
    }
  };
  auto pvk = [&](int ks) {
#pragma unroll
    for (int t = 0; t < 2; ++t)
#pragma unroll
      for (int j = 0; j < 3; ++j) oa[t][j] = mfma(vf[ks][t], pf[ks][j], oa[t][j]);
  };
  auto rescale = [&]() {
#pragma unroll
    for (int t = 0; t < 2; ++t)
#pragma unroll
      for (int j = 0; j < 3; ++j) oa[t][j] *= alpha[j];
  };
  auto expo1 = [&](int js, int j) {
    const float nm = -mrun[j] * CS;
    float pe[8];
#pragma unroll
    for (int e = 0; e < 8; ++e) pe[e] = __builtin_amdgcn_exp2f(fmaf(sc[e >> 2][j][e & 3], CS, nm));
    const u32x4 pk = {cvtpk(pe[0], pe[1]), cvtpk(pe[2], pe[3]), cvtpk(pe[4], pe[5]), cvtpk(pe[6], pe[7])};
    if (js > 0) lacc[j][0] *= alpha[j];
    lacc[j] = mfma(ones, __builtin_bit_cast(bf16x8, pk), lacc[j]);
    *(LAS u32x4*)(uintptr_t)(lb + XOFF + (unsigned)(js & 1) * PBUF + (w * 3u + j) * 1024u + lane * 16u) = pk;
  };
#define OPEN(n) do { if ((n) == 0) waitv<16>(); else if ((n) == 1) waitv<8>(); else waitv<4>(); BARRIER(); STAMP(10 + (n)); } while (0)
#define AINT(js) do { \
    OPEN(2 * (js)); \
    if ((js) == 0) { \
      _Pragma("unroll") for (int s = 0; s < 4; ++s) { rdk(8, 0, s); \
        _Pragma("unroll") for (int j = 0; j < 3; ++j) { const unsigned row = r * 48u + 16u * j + li; \
          qf[j][s] = lds_b128(lb + XOFF + row * 256u + (((4u * s + g) ^ (row & 15u)) * 16u)); } } \
      _Pragma("unroll") for (int s = 0; s < 4; ++s) rdk(8, 1, s); \
      SB(); } \
    _Pragma("unroll") for (int kt = 0; kt < 2; ++kt) { \
      _Pragma("unroll") for (int j = 0; j < 3; ++j) sc[kt][j] = f32x4{0.f, 0.f, 0.f, 0.f}; \
      _Pragma("unroll") for (int s = 0; s < 4; ++s) { \
        _Pragma("unroll") for (int j = 0; j < 3; ++j) sc[kt][j] = mfma(ka[kt][s], qf[j][s], sc[kt][j]); \
        SB(); \
        if ((js) >= 1) { if (kt == 0) { rdv(8 + 2 * (js), s); piece(10 + 2 * (js), w * 4u, s); } else rdp((js) - 1, s); } \
        if ((js) >= 2 && s < 3) oa[kt][s] *= alpha[s]; \
        SB(); \
      } } \
    _Pragma("unroll") for (int j = 0; j < 3; ++j) { \
      const float m = max2f(max3f(sc[0][j][0], sc[0][j][1], sc[0][j][2]), max3f(max3f(sc[0][j][3], sc[1][j][0], sc[1][j][1]), sc[1][j][2], sc[1][j][3])); \
      lds_fmax(lb + SMAXOFF + (r * 48u + 16u * j + li) * 4u, m); } \
  } while (0)
#define BINT(js) do { \
    OPEN(2 * (js) + 1); \
    _Pragma("unroll") for (int j = 0; j < 3; ++j) { \
      const float m = smax[r * 48u + 16u * j + li]; \
      if ((js) > 0) alpha[j] = __builtin_amdgcn_exp2f((mrun[j] - m) * CS); \
      mrun[j] = m; } \
    _Pragma("unroll") for (int ks = 0; ks < 4; ++ks) { \
      if ((js) >= 1) pvk(ks); \
      if (ks < 3) expo1(js, ks); \
      if ((js) >= 1 && ks < 3) { _Pragma("unroll") for (int z = 0; z < 6; ++z) { __builtin_amdgcn_sched_group_barrier(0x8, 1, 0); __builtin_amdgcn_sched_group_barrier(0x402, 4, 0); } } \
      SB(); \
      if ((js) < 3) { rdk(9 + 2 * (js), 0, ks); rdk(9 + 2 * (js), 1, ks); } else rdv(15, ks); \
      piece(11 + 2 * (js), w * 4u, ks); \
      SB(); \
    } \
  } while (0)
  AINT(0); BINT(0); AINT(1); BINT(1); AINT(2); BINT(2); AINT(3); BINT(3);
#pragma unroll
  for (int j = 0; j < 3; ++j) { ssum[(r * 4u + c) * 48u + 16u * j + li] = lacc[j][0]; }
  OPEN(8);
#pragma unroll
  for (int ks = 0; ks < 4; ++ks) rdp(3, ks);
  rescale();
  float inv[3];
#pragma unroll
  for (int j = 0; j < 3; ++j)
    inv[j] = __builtin_amdgcn_rcpf((ssum[(r * 4u + 0u) * 48u + 16u * j + li] + ssum[(r * 4u + 1u) * 48u + 16u * j + li]) +
                                   (ssum[(r * 4u + 2u) * 48u + 16u * j + li] + ssum[(r * 4u + 3u) * 48u + 16u * j + li]));
#pragma unroll
  for (int ks = 0; ks < 4; ++ks) {
    pvk(ks);
    SB();
    piece(18, w * 4u, ks);
    if (!loader) rdw(0, ks);
    SB();
  }
  STAMP(19);
#pragma unroll
  for (int j = 0; j < 3; ++j)
#pragma unroll
    for (int t = 0; t < 2; ++t) {
      const unsigned row = 16u * j + li;
      const unsigned k = r * 128u + 32u * c + 16u * t + 4u * g;
      u32x2 v = {cvtpk(oa[t][j][0] * inv[j], oa[t][j][1] * inv[j]), cvtpk(oa[t][j][2] * inv[j], oa[t][j][3] * inv[j])};
      *(LAS u32x2*)(uintptr_t)(lb + XOFF + row * 512u + (((k >> 3) ^ li) * 16u) + (k & 7u) * 2u) = v;
    }
  STAMP(20); STAMP(21); STAMP(22);

  if (loader) {
#define L3STEP(e) do { if ((e) == 0) waitv<4>(); else if ((e) < 6) waitv<8>(); else waitv<0>(); BARRIER(); STAMP(23 + (e)); if ((e) + 3 < 8) issue8(19 + (e)); } while (0)
    L3STEP(0); L3STEP(1); L3STEP(2); L3STEP(3); L3STEP(4); L3STEP(5); L3STEP(6);
    BARRIER();
    if (c == 0) {
      for (unsigned spin = 0; spin < (1u << 20); ++spin) {
        const unsigned long long f = __hip_atomic_load(flags + hp * 16u + li, __ATOMIC_RELAXED, __HIP_MEMORY_SCOPE_AGENT);
        if (__all(f == tag)) break;
        __builtin_amdgcn_s_sleep(POLLSLEEP);
      }
    }
    BARRIER();
    if (c < 3) {
      STAMP(30);
      u32x4 v[16];
#pragma unroll
      for (int p = 0; p < 16; ++p) v[p] = __builtin_amdgcn_raw_buffer_load_b128(rp, ((hp * 3u + c) * 64u + lane) * 16u, p * (SQ * DM * 2), 16);
      float sum[8];
#pragma unroll
      for (int k = 0; k < 8; ++k) sum[k] = 0.f;
#pragma unroll
      for (int p = 0; p < 16; ++p)
#pragma unroll
        for (int k = 0; k < 4; ++k) {
          sum[2 * k] += __uint_as_float(v[p][k] << 16);
          sum[2 * k + 1] += __uint_as_float(v[p][k] & 0xffff0000u);
        }
      unsigned short* o = out + ((size_t)b * SQ + 16u * c + li) * DM + 32u * hp + 4u * g;
      *(u32x2*)o = u32x2{cvtpk(sum[0], sum[1]), cvtpk(sum[2], sum[3])};
      *(u32x2*)(o + 16) = u32x2{cvtpk(sum[4], sum[5]), cvtpk(sum[6], sum[7])};
      if (hp == 0 && c == 0 && lane == 0) __hip_atomic_store(epoch, tag, __ATOMIC_RELAXED, __HIP_MEMORY_SCOPE_AGENT);
    }
  } else {
    bf16x8 of[3][8];
    f32x4 pa[2][3];
#define C3STEP(e) do { \
      if ((e) < 7) { if ((e) == 0) waitv<4>(); else if ((e) == 1) waitv<0>(); BARRIER(); } STAMP(23 + (e)); \
      if ((e) == 0) { \
        _Pragma("unroll") for (int s = 0; s < 8; ++s) \
        _Pragma("unroll") for (int j = 0; j < 3; ++j) of[j][s] = lds_b128(lb + XOFF + (16u * j + li) * 512u + (((4u * s + g) ^ li) * 16u)); } \
      if (!((e) & 1)) { _Pragma("unroll") for (int j = 0; j < 3; ++j) { pa[0][j] = f32x4{0.f, 0.f, 0.f, 0.f}; pa[1][j] = f32x4{0.f, 0.f, 0.f, 0.f}; } } \
      SB(); \
      _Pragma("unroll") for (int ks = 0; ks < 4; ++ks) { \
        _Pragma("unroll") for (int t = 0; t < 2; ++t) \
        _Pragma("unroll") for (int j = 0; j < 3; ++j) pa[t][j] = mfma(wf[ks][t], of[j][4 * ((e) & 1) + ks], pa[t][j]); \
        SB(); if ((e) + 1 < 8) rdw((e) + 1, ks); SB(); \
      } \
      if ((e) & 1) { const unsigned nb = (((unsigned)(e) >> 1) + nrot) & 3u; \
        _Pragma("unroll") for (int j = 0; j < 3; ++j) \
          __builtin_amdgcn_raw_buffer_store_b128(u32x4{cvtpk(pa[0][j][0], pa[0][j][1]), cvtpk(pa[0][j][2], pa[0][j][3]), cvtpk(pa[1][j][0], pa[1][j][1]), cvtpk(pa[1][j][2], pa[1][j][3])}, \
                                                 rp, lane * 16u + 1024u * j, hp * (SQ * DM * 2) + (nb * 4u + c) * 3072u, 16); \
        if ((e) == 7) { STAMP(33); waitv<0>(); STAMP(36); \
          if (lane < 4u) __hip_atomic_store(flags + (lane * 4u + c) * 16u + hp, tag, __ATOMIC_RELAXED, __HIP_MEMORY_SCOPE_AGENT); } } \
    } while (0)
    C3STEP(0); C3STEP(1); C3STEP(2); C3STEP(3); C3STEP(4); C3STEP(5); C3STEP(6); C3STEP(7);
    BARRIER(); BARRIER();
  }
}

extern "C" void kernel_launch(void* const* d_in, const int* in_sizes, int n_in,
                              void* d_out, int out_size, void* d_ws, size_t ws_size,
                              hipStream_t stream) {
  (void)in_sizes; (void)n_in; (void)out_size; (void)ws_size;
  (void)hipFuncSetAttribute((const void*)gqa_fused, hipFuncAttributeMaxDynamicSharedMemorySize, LDS_BYTES);
  const unsigned short* x = (const unsigned short*)d_in[0];
  const unsigned short* kc = (const unsigned short*)d_in[1];
  const unsigned short* vc = (const unsigned short*)d_in[2];
  const unsigned short* wq = (const unsigned short*)d_in[3];
  const unsigned short* wo = (const unsigned short*)d_in[4];
  unsigned short* part = (unsigned short*)d_ws;
  hipLaunchKernelGGL(gqa_fused, dim3(256), dim3(512), LDS_BYTES, stream, x, kc, vc, wq, wo, part, (unsigned short*)d_out);
}
```

```cpp
#include <hip/hip_runtime.h>
#include <hip/hip_bf16.h>
#include <stdint.h>

typedef short bf16x8 __attribute__((ext_vector_type(8)));
typedef short bf16x4 __attribute__((ext_vector_type(4)));
typedef float f32x4 __attribute__((ext_vector_type(4)));
typedef float f32x2 __attribute__((ext_vector_type(2)));
typedef __bf16 bf2 __attribute__((ext_vector_type(2)));
typedef unsigned u32x2 __attribute__((ext_vector_type(2)));
typedef unsigned u32x4 __attribute__((ext_vector_type(4)));
#define LAS __attribute__((address_space(3)))
#define DI __device__ __forceinline__

constexpr int NB = 16, SQ = 48, DM = 512, HQ = 32, HKV = 8, SK = 512, DH = 128;
constexpr unsigned XOFF = 0;
constexpr unsigned ROFF = 65536;
constexpr unsigned SLOT = 32768;
constexpr unsigned PBUF = 24576;
constexpr unsigned SMAXOFF = 49152, SSUMOFF = 51200;
constexpr unsigned LDS_BYTES = 163840;
#define POLLSLEEP 4
constexpr size_t FLAG_OFF = 16u << 20, EPOCH_OFF = 17u << 20;
typedef __attribute__((address_space(1))) unsigned long long gu64;

DI unsigned rfl(unsigned x) { return __builtin_amdgcn_readfirstlane(x); }
DI unsigned cvtpk(float lo, float hi) {
  f32x2 v = {lo, hi};
  bf2 b = __builtin_convertvector(v, bf2);
  return __builtin_bit_cast(unsigned, b);
}
#define BARRIER() asm volatile("s_waitcnt lgkmcnt(0)\n\ts_barrier" ::: "memory")
#define SB() __builtin_amdgcn_sched_barrier(0)
template <int N> DI void waitv() { asm volatile("s_waitcnt vmcnt(%0)" :: "i"(N > 63 ? 63 : N) : "memory"); }
template <int NT> DI void dma(__amdgpu_buffer_rsrc_t rsrc, unsigned voff, unsigned soff, unsigned ldsdst) {
  if (NT) asm volatile("s_mov_b32 m0, %0\n\ts_nop 0\n\tbuffer_load_dwordx4 %1, %2, %3 offen nt lds"
               :: "s"(ldsdst), "v"(voff), "s"(rsrc), "s"(soff) : "memory");
  else asm volatile("s_mov_b32 m0, %0\n\ts_nop 0\n\tbuffer_load_dwordx4 %1, %2, %3 offen lds"
               :: "s"(ldsdst), "v"(voff), "s"(rsrc), "s"(soff) : "memory");
}
DI bf16x8 lds_b128(unsigned a) { return *(const LAS bf16x8*)(uintptr_t)a; }
DI bf16x4 lds_tr(unsigned a) { return __builtin_amdgcn_ds_read_tr16_b64_v4i16((LAS bf16x4*)(uintptr_t)a); }
DI bf16x8 cat(bf16x4 a, bf16x4 b) { return __builtin_shufflevector(a, b, 0, 1, 2, 3, 4, 5, 6, 7); }
DI f32x4 mfma(bf16x8 a, bf16x8 b, f32x4 c) { return __builtin_amdgcn_mfma_f32_16x16x32_bf16(a, b, c, 0, 0, 0); }
DI unsigned fsw(unsigned row) { return (row & 3u) | (((row >> 3) & 1u) << 2); }
DI float max2f(float a, float b) { return __builtin_elementwise_maximum(a, b); }
DI float max3f(float a, float b, float c) { return max2f(max2f(a, b), c); }
DI void lds_fmax(unsigned a, float v) { asm volatile("ds_max_f32 %0, %1" :: "v"(a), "v"(v) : "memory"); }
DI float xmax4(float v) {
  auto a = __builtin_amdgcn_permlane32_swap(__float_as_uint(v), __float_as_uint(v), false, false);
  v = fmaxf(__uint_as_float(a[0]), __uint_as_float(a[1]));
  auto b = __builtin_amdgcn_permlane16_swap(__float_as_uint(v), __float_as_uint(v), false, false);
  return fmaxf(__uint_as_float(b[0]), __uint_as_float(b[1]));
}

__global__ __launch_bounds__(512, 2)
void gqa_fused(const unsigned short* __restrict__ x, const unsigned short* __restrict__ kc,
               const unsigned short* __restrict__ vc, const unsigned short* __restrict__ wq,
               const unsigned short* __restrict__ wo, unsigned short* __restrict__ part,
               unsigned short* __restrict__ out) {
  extern __shared__ __attribute__((aligned(16))) char smem[];
  const unsigned lb = (unsigned)(uintptr_t)(LAS char*)smem;
  const unsigned tid = threadIdx.x, lane = tid & 63u;
  const unsigned w = rfl(tid >> 6);
  const unsigned bid = blockIdx.x, hkv = bid & 7u, idx = bid >> 3, b = idx >> 1, half = idx & 1u;
  const unsigned h0 = hkv * 4u + half * 2u;
  const unsigned g = lane >> 4, li = lane & 15u, q4 = li >> 2, p4 = lane & 3u;
  const unsigned r = w >> 2, c = w & 3u;
  const unsigned rot = b & 7u, nrot = b & 3u;
  const bool loader = (r == 0);

  const __amdgpu_buffer_rsrc_t rx = __builtin_amdgcn_make_buffer_rsrc((void*)(x + (size_t)b * SQ * DM), (short)0, SQ * DM * 2, 0x00020000);
  const __amdgpu_buffer_rsrc_t rwq = __builtin_amdgcn_make_buffer_rsrc((void*)wq, (short)0, DM * HQ * DH * 2, 0x00020000);
  const __amdgpu_buffer_rsrc_t rwo = __builtin_amdgcn_make_buffer_rsrc((void*)wo, (short)0, DM * HQ * DH * 2, 0x00020000);
  const __amdgpu_buffer_rsrc_t rk = __builtin_amdgcn_make_buffer_rsrc((void*)(kc + ((size_t)b * HKV + hkv) * SK * DH), (short)0, SK * DH * 2, 0x00020000);
  const __amdgpu_buffer_rsrc_t rv = __builtin_amdgcn_make_buffer_rsrc((void*)(vc + ((size_t)b * HKV + hkv) * SK * DH), (short)0, SK * DH * 2, 0x00020000);
  const unsigned hp = hkv * 2u + half;
  const __amdgpu_buffer_rsrc_t rp = __builtin_amdgcn_make_buffer_rsrc((void*)(part + (size_t)b * 16u * (SQ * DM)), (short)0, 16 * SQ * DM * 2, 0x00020000);
  gu64* flags = (gu64*)((char*)part + FLAG_OFF) + (size_t)b * 256u;
  gu64* epoch = (gu64*)((char*)part + EPOCH_OFF) + (size_t)b * 16u;
  unsigned long long tag;
  auto gettag = [&]() {
    unsigned long long e;
    asm volatile("s_load_dwordx2 %0, %1, 0x0\n\ts_waitcnt lgkmcnt(0)" : "=&s"(e) : "s"(epoch) : "memory");
    tag = e * 0x9E3779B97F4A7C15ull + 0xD1B54A32D192ED03ull;
  };

  auto piece = [&](int t, unsigned pb, int i) {
    const unsigned dst = lb + ROFF + (unsigned)(t % 3) * SLOT + pb * 1024u + 1024u * i;
    if (t < 8) {
      const unsigned kb = ((unsigned)t + rot) & 7u;
      const unsigned row = 2u * i + (lane >> 5);
      const unsigned ch = (lane & 31u) ^ (fsw(row) << 1);
      dma<0>(rwq, row * 8192u + ch * 16u, (kb * 64u + pb * 2u) * 8192u + h0 * 256u, dst);
    } else if (t < 16) {
      const bool isk = (t == 8) || (t & 1 && t < 15);
      const int blk = t == 8 ? 0 : (t == 15 ? 3 : (isk ? (t - 7) / 2 : (t - 10) / 2));
      const unsigned row = 4u * i + g;
      const unsigned ch = isk ? (li ^ (row & 15u)) : (li ^ ((row & 7u) << 1));
      dma<1>(isk ? rk : rv, row * 256u + ch * 16u, (unsigned)blk * 32768u + pb * 1024u, dst);
    } else {
      const unsigned nb = (((unsigned)(t - 16) >> 1) + nrot) & 3u, kh = (unsigned)(t - 16) & 1u;
      const unsigned row = 4u * i + g;
      const unsigned ch = li ^ (fsw(row) << 1);
      dma<0>(rwo, row * 1024u + ch * 16u, (h0 * 128u + kh * 128u + pb * 4u) * 1024u + nb * 256u, dst);
    }
  };
  auto issue8 = [&](int t) {
#pragma unroll
    for (int i = 0; i < 8; ++i) piece(t, c * 8u, i);
  };

  if (loader) {
#pragma unroll
    for (int i = 0; i < 12; ++i) {
      const unsigned row = c * 12u + i;
      const unsigned ch = lane ^ (row & 15u);
      dma<0>(rx, ch * 16u, row * 1024u, lb + XOFF + row * 1024u);
    }
    issue8(0); issue8(1); issue8(2);
    if (tid < 96u) *(LAS float*)(uintptr_t)(lb + SMAXOFF + tid * 4u) = -__builtin_inff();
    gettag();
    waitv<16>();
    BARRIER();
#define L1STEP(d) do { if ((d) < 7) waitv<8>(); BARRIER(); issue8((d) + 3); } while (0)
    L1STEP(0); L1STEP(1); L1STEP(2); L1STEP(3); L1STEP(4); L1STEP(5); L1STEP(6); L1STEP(7);
  } else {
    gettag();
    f32x4 qa[4][3];
#pragma unroll
    for (int t = 0; t < 4; ++t)
#pragma unroll
      for (int j = 0; j < 3; ++j) qa[t][j] = f32x4{0.f, 0.f, 0.f, 0.f};
    bf16x8 fa[2][4], fb[2][3];
    auto rda = [&](int d, int ks, int t) {
      const unsigned sb = lb + ROFF + (unsigned)(d % 3) * SLOT;
      bf16x4 h[2];
#pragma unroll
      for (int u = 0; u < 2; ++u) {
        const unsigned row = 32u * ks + 8u * g + 4u * u + q4;
        const unsigned ch = (8u * c + 2u * t + (p4 >> 1)) ^ (fsw(row) << 1);
        h[u] = lds_tr(sb + row * 512u + ch * 16u + 8u * (p4 & 1u));
      }
      fa[ks][t] = cat(h[0], h[1]);
    };
    auto rdb = [&](int d, int ks) {
      const unsigned kb = ((unsigned)d + rot) & 7u;
#pragma unroll
      for (int j = 0; j < 3; ++j) {
        const unsigned row = 16u * j + li;
        const unsigned ch = (8u * kb + 4u * ks + g) ^ li;
        fb[ks][j] = lds_b128(lb + XOFF + row * 1024u + ch * 16u);
      }
    };
    BARRIER();
#pragma unroll
    for (int ks = 0; ks < 2; ++ks) { rda(0, ks, 0); rdb(0, ks); rda(0, ks, 1); rda(0, ks, 2); rda(0, ks, 3); }
#define C1STEP(d) do { \
      BARRIER(); \
      _Pragma("unroll") for (int q = 0; q < 8; ++q) { \
        _Pragma("unroll") for (int j = 0; j < 3; ++j) qa[q & 3][j] = mfma(fa[q >> 2][q & 3], fb[q >> 2][j], qa[q & 3][j]); \
        SB(); \
        if ((d) + 1 < 8) { rda((d) + 1, q >> 2, q & 3); if ((q & 3) == 3) rdb((d) + 1, q >> 2); } \
        SB(); \
      } \
    } while (0)
    C1STEP(0); C1STEP(1); C1STEP(2); C1STEP(3); C1STEP(4); C1STEP(5); C1STEP(6); C1STEP(7);
#pragma unroll
    for (int t = 0; t < 4; ++t)
#pragma unroll
      for (int j = 0; j < 3; ++j) {
        const unsigned row = (c >> 1) * 48u + 16u * j + li;
        const unsigned d = 64u * (c & 1u) + 16u * t + 4u * g;
        u32x2 v = {cvtpk(qa[t][j][0], qa[t][j][1]), cvtpk(qa[t][j][2], qa[t][j][3])};
        *(LAS u32x2*)(uintptr_t)(lb + XOFF + row * 256u + (((d >> 3) ^ (row & 15u)) * 16u) + (d & 7u) * 2u) = v;
      }
  }

  const float CS = 0.088388347648318447f * 1.4426950408889634f;
  LAS float* smax = (LAS float*)(uintptr_t)(lb + SMAXOFF);
  LAS float* ssum = (LAS float*)(uintptr_t)(lb + SSUMOFF);
  bf16x8 qf[3][4], ka[2][4], vf[4][2], pf[4][3], wf[4][2];
  f32x4 sc[2][3], oa[2][3], lacc[3];
  float mrun[3], alpha[3];
#pragma unroll
  for (int j = 0; j < 3; ++j) {
    lacc[j] = f32x4{0.f, 0.f, 0.f, 0.f};
    oa[0][j] = f32x4{0.f, 0.f, 0.f, 0.f};
    oa[1][j] = f32x4{0.f, 0.f, 0.f, 0.f};
    mrun[j] = 0.f; alpha[j] = 1.f;
  }
  const bf16x8 ones = {0x3f80, 0x3f80, 0x3f80, 0x3f80, 0x3f80, 0x3f80, 0x3f80, 0x3f80};
  auto rdk = [&](int t, int kt, int s) {
    ka[kt][s] = lds_b128(lb + ROFF + (unsigned)(t % 3) * SLOT + (32u * c + 16u * kt + li) * 256u + (((4u * s + g) ^ li) * 16u));
  };
  auto rdv = [&](int t, int ks) {
    const unsigned row0 = 4u * g + q4;
    const unsigned a0 = lb + ROFF + (unsigned)(t % 3) * SLOT + row0 * 256u + ((((row0 & 7u) ^ (2u * c)) << 5)) + (p4 << 3);
#pragma unroll
    for (int tt = 0; tt < 2; ++tt) vf[ks][tt] = cat(lds_tr((a0 ^ (unsigned)(tt << 5)) + 8192u * ks), lds_tr((a0 ^ (unsigned)(tt << 5)) + 8192u * ks + 4096u));
  };
  auto rdp = [&](int js, int ks) {
    const unsigned pb = lb + XOFF + (unsigned)(js & 1) * PBUF + r * 12288u + lane * 16u;
#pragma unroll
    for (int j = 0; j < 3; ++j) pf[ks][j] = lds_b128(pb + (unsigned)(ks * 3 + j) * 1024u);
  };
  auto rdw = [&](int e, int ks) {
    const unsigned sb = lb + ROFF + (unsigned)((16 + e) % 3) * SLOT;
#pragma unroll
    for (int t = 0; t < 2; ++t) {
      bf16x4 h[2];
#pragma unroll
      for (int u = 0; u < 2; ++u) {
        const unsigned row = 32u * ks + 8u * g + 4u * u + q4;
        const unsigned ch = (4u * c + 2u * t + (p4 >> 1)) ^ (fsw(row) << 1);
        h[u] = lds_tr(sb + row * 256u + ch * 16u + 8u * (p4 & 1u));
      }
      wf[ks][t] = cat(h[0], h[1]);
    }
  };
  auto pvk = [&](int ks) {
#pragma unroll
    for (int t = 0; t < 2; ++t)
#pragma unroll
      for (int j = 0; j < 3; ++j) oa[t][j] = mfma(vf[ks][t], pf[ks][j], oa[t][j]);
  };
  auto rescale = [&]() {
#pragma unroll
    for (int t = 0; t < 2; ++t)
#pragma unroll
      for (int j = 0; j < 3; ++j) oa[t][j] *= alpha[j];
  };
  auto expo1 = [&](int js, int j) {
    const float nm = -mrun[j] * CS;
    float pe[8];
#pragma unroll
    for (int e = 0; e < 8; ++e) pe[e] = __builtin_amdgcn_exp2f(fmaf(sc[e >> 2][j][e & 3], CS, nm));
    const u32x4 pk = {cvtpk(pe[0], pe[1]), cvtpk(pe[2], pe[3]), cvtpk(pe[4], pe[5]), cvtpk(pe[6], pe[7])};
    if (js > 0) lacc[j][0] *= alpha[j];
    lacc[j] = mfma(ones, __builtin_bit_cast(bf16x8, pk), lacc[j]);
    *(LAS u32x4*)(uintptr_t)(lb + XOFF + (unsigned)(js & 1) * PBUF + (w * 3u + j) * 1024u + lane * 16u) = pk;
  };
#define OPEN(n) do { if ((n) == 0) waitv<16>(); else if ((n) == 1) waitv<8>(); else waitv<4>(); BARRIER(); } while (0)
#define AINT(js) do { \
    OPEN(2 * (js)); \
    if ((js) == 0) { \
      _Pragma("unroll") for (int s = 0; s < 4; ++s) { rdk(8, 0, s); \
        _Pragma("unroll") for (int j = 0; j < 3; ++j) { const unsigned row = r * 48u + 16u * j + li; \
          qf[j][s] = lds_b128(lb + XOFF + row * 256u + (((4u * s + g) ^ (row & 15u)) * 16u)); } } \
      _Pragma("unroll") for (int s = 0; s < 4; ++s) rdk(8, 1, s); \
      SB(); } \
    _Pragma("unroll") for (int kt = 0; kt < 2; ++kt) { \
      _Pragma("unroll") for (int j = 0; j < 3; ++j) sc[kt][j] = f32x4{0.f, 0.f, 0.f, 0.f}; \
      _Pragma("unroll") for (int s = 0; s < 4; ++s) { \
        _Pragma("unroll") for (int j = 0; j < 3; ++j) sc[kt][j] = mfma(ka[kt][s], qf[j][s], sc[kt][j]); \
        SB(); \
        if ((js) >= 1) { if (kt == 0) { rdv(8 + 2 * (js), s); piece(10 + 2 * (js), w * 4u, s); } else rdp((js) - 1, s); } \
        if ((js) >= 2 && s < 3) oa[kt][s] *= alpha[s]; \
        SB(); \
      } } \
    _Pragma("unroll") for (int j = 0; j < 3; ++j) { \
      const float m = max2f(max3f(sc[0][j][0], sc[0][j][1], sc[0][j][2]), max3f(max3f(sc[0][j][3], sc[1][j][0], sc[1][j][1]), sc[1][j][2], sc[1][j][3])); \
      lds_fmax(lb + SMAXOFF + (r * 48u + 16u * j + li) * 4u, m); } \
  } while (0)
#define BINT(js) do { \
    OPEN(2 * (js) + 1); \
    _Pragma("unroll") for (int j = 0; j < 3; ++j) { \
      const float m = smax[r * 48u + 16u * j + li]; \
      if ((js) > 0) alpha[j] = __builtin_amdgcn_exp2f((mrun[j] - m) * CS); \
      mrun[j] = m; } \
    _Pragma("unroll") for (int ks = 0; ks < 4; ++ks) { \
      if ((js) >= 1) pvk(ks); \
      if (ks < 3) expo1(js, ks); \
      if ((js) >= 1 && ks < 3) { _Pragma("unroll") for (int z = 0; z < 6; ++z) { __builtin_amdgcn_sched_group_barrier(0x8, 1, 0); __builtin_amdgcn_sched_group_barrier(0x402, 4, 0); } } \
      SB(); \
      if ((js) < 3) { rdk(9 + 2 * (js), 0, ks); rdk(9 + 2 * (js), 1, ks); } else rdv(15, ks); \
      piece(11 + 2 * (js), w * 4u, ks); \
      SB(); \
    } \
  } while (0)
  AINT(0); BINT(0); AINT(1); BINT(1); AINT(2); BINT(2); AINT(3); BINT(3);
#pragma unroll
  for (int j = 0; j < 3; ++j) { ssum[(r * 4u + c) * 48u + 16u * j + li] = lacc[j][0]; }
  OPEN(8);
#pragma unroll
  for (int ks = 0; ks < 4; ++ks) rdp(3, ks);
  rescale();
  float inv[3];
#pragma unroll
  for (int j = 0; j < 3; ++j)
    inv[j] = __builtin_amdgcn_rcpf((ssum[(r * 4u + 0u) * 48u + 16u * j + li] + ssum[(r * 4u + 1u) * 48u + 16u * j + li]) +
                                   (ssum[(r * 4u + 2u) * 48u + 16u * j + li] + ssum[(r * 4u + 3u) * 48u + 16u * j + li]));
#pragma unroll
  for (int ks = 0; ks < 4; ++ks) {
    pvk(ks);
    SB();
    piece(18, w * 4u, ks);
    if (!loader) rdw(0, ks);
    SB();
  }

#pragma unroll
  for (int j = 0; j < 3; ++j)
#pragma unroll
    for (int t = 0; t < 2; ++t) {
      const unsigned row = 16u * j + li;
      const unsigned k = r * 128u + 32u * c + 16u * t + 4u * g;
      u32x2 v = {cvtpk(oa[t][j][0] * inv[j], oa[t][j][1] * inv[j]), cvtpk(oa[t][j][2] * inv[j], oa[t][j][3] * inv[j])};
      *(LAS u32x2*)(uintptr_t)(lb + XOFF + row * 512u + (((k >> 3) ^ li) * 16u) + (k & 7u) * 2u) = v;
    }

  if (loader) {
#define L3STEP(e) do { if ((e) == 0) waitv<4>(); else if ((e) < 6) waitv<8>(); else waitv<0>(); BARRIER(); if ((e) + 3 < 8) issue8(19 + (e)); } while (0)
    L3STEP(0); L3STEP(1); L3STEP(2); L3STEP(3); L3STEP(4); L3STEP(5); L3STEP(6);
    BARRIER();
    if (c == 0) {
      for (unsigned spin = 0; spin < (1u << 17); ++spin) {
        const unsigned long long f = __hip_atomic_load(flags + hp * 16u + li, __ATOMIC_RELAXED, __HIP_MEMORY_SCOPE_AGENT);
        if (__all(f == tag)) break;
        __builtin_amdgcn_s_sleep(POLLSLEEP);
      }
    }
    BARRIER();
    if (c < 3) {

      u32x4 v[16];
#pragma unroll
      for (int p = 0; p < 16; ++p) v[p] = __builtin_amdgcn_raw_buffer_load_b128(rp, ((hp * 3u + c) * 64u + lane) * 16u, p * (SQ * DM * 2), 16);
      float sum[8];
#pragma unroll
      for (int k = 0; k < 8; ++k) sum[k] = 0.f;
#pragma unroll
      for (int p = 0; p < 16; ++p)
#pragma unroll
        for (int k = 0; k < 4; ++k) {
          sum[2 * k] += __uint_as_float(v[p][k] << 16);
          sum[2 * k + 1] += __uint_as_float(v[p][k] & 0xffff0000u);
        }
      unsigned short* o = out + ((size_t)b * SQ + 16u * c + li) * DM + 32u * hp + 4u * g;
      *(u32x2*)o = u32x2{cvtpk(sum[0], sum[1]), cvtpk(sum[2], sum[3])};
      *(u32x2*)(o + 16) = u32x2{cvtpk(sum[4], sum[5]), cvtpk(sum[6], sum[7])};
      if (hp == 0 && c == 0 && lane == 0) __hip_atomic_store(epoch, tag, __ATOMIC_RELAXED, __HIP_MEMORY_SCOPE_AGENT);
    }
  } else {
    bf16x8 of[3][8];
    f32x4 pa[2][3];
#define C3STEP(e) do { \
      if ((e) < 7) { if ((e) == 0) waitv<4>(); else if ((e) == 1) waitv<0>(); BARRIER(); } \
      if ((e) == 0) { \
        _Pragma("unroll") for (int s = 0; s < 8; ++s) \
        _Pragma("unroll") for (int j = 0; j < 3; ++j) of[j][s] = lds_b128(lb + XOFF + (16u * j + li) * 512u + (((4u * s + g) ^ li) * 16u)); } \
      if (!((e) & 1)) { _Pragma("unroll") for (int j = 0; j < 3; ++j) { pa[0][j] = f32x4{0.f, 0.f, 0.f, 0.f}; pa[1][j] = f32x4{0.f, 0.f, 0.f, 0.f}; } } \
      SB(); \
      _Pragma("unroll") for (int ks = 0; ks < 4; ++ks) { \
        _Pragma("unroll") for (int t = 0; t < 2; ++t) \
        _Pragma("unroll") for (int j = 0; j < 3; ++j) pa[t][j] = mfma(wf[ks][t], of[j][4 * ((e) & 1) + ks], pa[t][j]); \
        SB(); if ((e) + 1 < 8) rdw((e) + 1, ks); SB(); \
      } \
      if ((e) & 1) { const unsigned nb = (((unsigned)(e) >> 1) + nrot) & 3u; \
        _Pragma("unroll") for (int j = 0; j < 3; ++j) \
          __builtin_amdgcn_raw_buffer_store_b128(u32x4{cvtpk(pa[0][j][0], pa[0][j][1]), cvtpk(pa[0][j][2], pa[0][j][3]), cvtpk(pa[1][j][0], pa[1][j][1]), cvtpk(pa[1][j][2], pa[1][j][3])}, \
                                                 rp, lane * 16u + 1024u * j, hp * (SQ * DM * 2) + (nb * 4u + c) * 3072u, 16); \
        if ((e) == 7) { waitv<0>(); \
          if (lane < 4u) __hip_atomic_store(flags + (lane * 4u + c) * 16u + hp, tag, __ATOMIC_RELAXED, __HIP_MEMORY_SCOPE_AGENT); } } \
    } while (0)
    C3STEP(0); C3STEP(1); C3STEP(2); C3STEP(3); C3STEP(4); C3STEP(5); C3STEP(6); C3STEP(7);
    BARRIER(); BARRIER();
  }
}

extern "C" void kernel_launch(void* const* d_in, const int* in_sizes, int n_in,
                              void* d_out, int out_size, void* d_ws, size_t ws_size,
                              hipStream_t stream) {
  (void)in_sizes; (void)n_in; (void)out_size; (void)ws_size;
  (void)hipFuncSetAttribute((const void*)gqa_fused, hipFuncAttributeMaxDynamicSharedMemorySize, LDS_BYTES);
  const unsigned short* x = (const unsigned short*)d_in[0];
  const unsigned short* kc = (const unsigned short*)d_in[1];
  const unsigned short* vc = (const unsigned short*)d_in[2];
  const unsigned short* wq = (const unsigned short*)d_in[3];
  const unsigned short* wo = (const unsigned short*)d_in[4];
  unsigned short* part = (unsigned short*)d_ws;
  hipLaunchKernelGGL(gqa_fused, dim3(256), dim3(512), LDS_BYTES, stream, x, kc, vc, wq, wo, part, (unsigned short*)d_out);
}
```

```cpp
#include <hip/hip_runtime.h>
#include <hip/hip_bf16.h>
#include <stdint.h>

typedef short bf16x8 __attribute__((ext_vector_type(8)));
typedef short bf16x4 __attribute__((ext_vector_type(4)));
typedef float f32x4 __attribute__((ext_vector_type(4)));
typedef float f32x2 __attribute__((ext_vector_type(2)));
typedef __bf16 bf2 __attribute__((ext_vector_type(2)));
typedef unsigned u32x2 __attribute__((ext_vector_type(2)));
typedef unsigned u32x4 __attribute__((ext_vector_type(4)));
#define LAS __attribute__((address_space(3)))
#define DI __device__ __forceinline__

constexpr int NB = 16, SQ = 48, DM = 512, HQ = 32, HKV = 8, SK = 512, DH = 128;
constexpr unsigned XOFF = 0;
constexpr unsigned ROFF = 65536;
constexpr unsigned SLOT = 32768;
constexpr unsigned PBUF = 24576;
constexpr unsigned SMAXOFF = 49152, SSUMOFF = 51200;
constexpr unsigned LDS_BYTES = 163840;
#define POLLSLEEP 4
constexpr size_t FLAG_OFF = 16u << 20, EPOCH_OFF = 17u << 20;
typedef __attribute__((address_space(1))) unsigned long long gu64;

DI unsigned rfl(unsigned x) { return __builtin_amdgcn_readfirstlane(x); }
DI unsigned cvtpk(float lo, float hi) {
  f32x2 v = {lo, hi};
  bf2 b = __builtin_convertvector(v, bf2);
  return __builtin_bit_cast(unsigned, b);
}
#define BARRIER() asm volatile("s_waitcnt lgkmcnt(0)\n\ts_barrier" ::: "memory")
#define SB() __builtin_amdgcn_sched_barrier(0)
template <int N> DI void waitv() { asm volatile("s_waitcnt vmcnt(%0)" :: "i"(N > 63 ? 63 : N) : "memory"); }
template <int NT> DI void dma(__amdgpu_buffer_rsrc_t rsrc, unsigned voff, unsigned soff, unsigned ldsdst) {
  if (NT) asm volatile("s_mov_b32 m0, %0\n\ts_nop 0\n\tbuffer_load_dwordx4 %1, %2, %3 offen nt lds"
               :: "s"(ldsdst), "v"(voff), "s"(rsrc), "s"(soff) : "memory");
  else asm volatile("s_mov_b32 m0, %0\n\ts_nop 0\n\tbuffer_load_dwordx4 %1, %2, %3 offen lds"
               :: "s"(ldsdst), "v"(voff), "s"(rsrc), "s"(soff) : "memory");
}
DI bf16x8 lds_b128(unsigned a) { return *(const LAS bf16x8*)(uintptr_t)a; }
DI bf16x4 lds_tr(unsigned a) { return __builtin_amdgcn_ds_read_tr16_b64_v4i16((LAS bf16x4*)(uintptr_t)a); }
DI bf16x8 cat(bf16x4 a, bf16x4 b) { return __builtin_shufflevector(a, b, 0, 1, 2, 3, 4, 5, 6, 7); }
DI f32x4 mfma(bf16x8 a, bf16x8 b, f32x4 c) { return __builtin_amdgcn_mfma_f32_16x16x32_bf16(a, b, c, 0, 0, 0); }
DI unsigned fsw(unsigned row) { return (row & 3u) | (((row >> 3) & 1u) << 2); }
DI float max2f(float a, float b) { return __builtin_elementwise_maximum(a, b); }
DI float max3f(float a, float b, float c) { return max2f(max2f(a, b), c); }
DI void lds_fmax(unsigned a, float v) { asm volatile("ds_max_f32 %0, %1" :: "v"(a), "v"(v) : "memory"); }
DI float xmax4(float v) {
  auto a = __builtin_amdgcn_permlane32_swap(__float_as_uint(v), __float_as_uint(v), false, false);
  v = fmaxf(__uint_as_float(a[0]), __uint_as_float(a[1]));
  auto b = __builtin_amdgcn_permlane16_swap(__float_as_uint(v), __float_as_uint(v), false, false);
  return fmaxf(__uint_as_float(b[0]), __uint_as_float(b[1]));
}

__global__ __launch_bounds__(512, 2)
void gqa_fused(const unsigned short* __restrict__ x, const unsigned short* __restrict__ kc,
               const unsigned short* __restrict__ vc, const unsigned short* __restrict__ wq,
               const unsigned short* __restrict__ wo, unsigned short* __restrict__ part,
               unsigned short* __restrict__ out) {
  extern __shared__ __attribute__((aligned(16))) char smem[];
  const unsigned lb = (unsigned)(uintptr_t)(LAS char*)smem;
  const unsigned tid = threadIdx.x, lane = tid & 63u;
  const unsigned w = rfl(tid >> 6);
  const unsigned bid = blockIdx.x, hkv = bid & 7u, idx = bid >> 3, b = idx >> 1, half = idx & 1u;
  const unsigned h0 = hkv * 4u + half * 2u;
  const unsigned g = lane >> 4, li = lane & 15u, q4 = li >> 2, p4 = lane & 3u;
  const unsigned r = w >> 2, c = w & 3u;
  const unsigned rot = b & 7u, nrot = b & 3u;
  const bool loader = (r == 0);

  const __amdgpu_buffer_rsrc_t rx = __builtin_amdgcn_make_buffer_rsrc((void*)(x + (size_t)b * SQ * DM), (short)0, SQ * DM * 2, 0x00020000);
  const __amdgpu_buffer_rsrc_t rwq = __builtin_amdgcn_make_buffer_rsrc((void*)wq, (short)0, DM * HQ * DH * 2, 0x00020000);
  const __amdgpu_buffer_rsrc_t rwo = __builtin_amdgcn_make_buffer_rsrc((void*)wo, (short)0, DM * HQ * DH * 2, 0x00020000);
  const __amdgpu_buffer_rsrc_t rk = __builtin_amdgcn_make_buffer_rsrc((void*)(kc + ((size_t)b * HKV + hkv) * SK * DH), (short)0, SK * DH * 2, 0x00020000);
  const __amdgpu_buffer_rsrc_t rv = __builtin_amdgcn_make_buffer_rsrc((void*)(vc + ((size_t)b * HKV + hkv) * SK * DH), (short)0, SK * DH * 2, 0x00020000);
  const unsigned hp = hkv * 2u + half;
  const __amdgpu_buffer_rsrc_t rp = __builtin_amdgcn_make_buffer_rsrc((void*)(part + (size_t)b * 16u * (SQ * DM)), (short)0, 16 * SQ * DM * 2, 0x00020000);
  gu64* flags = (gu64*)((char*)part + FLAG_OFF) + (size_t)b * 256u;
  gu64* epoch = (gu64*)((char*)part + EPOCH_OFF) + (size_t)b * 16u;
  unsigned long long tag;
  auto gettag = [&]() {
    unsigned long long e;
    asm volatile("s_load_dwordx2 %0, %1, 0x0\n\ts_waitcnt lgkmcnt(0)" : "=&s"(e) : "s"(epoch) : "memory");
    tag = e * 0x9E3779B97F4A7C15ull + 0xD1B54A32D192ED03ull;
  };

  auto piece = [&](int t, unsigned pb, int i) {
    const unsigned dst = lb + ROFF + (unsigned)(t % 3) * SLOT + pb * 1024u + 1024u * i;
    if (t < 8) {
      const unsigned kb = ((unsigned)t + rot) & 7u;
      const unsigned row = 2u * i + (lane >> 5);
      const unsigned ch = (lane & 31u) ^ (fsw(row) << 1);
      dma<0>(rwq, row * 8192u + ch * 16u, (kb * 64u + pb * 2u) * 8192u + h0 * 256u, dst);
    } else if (t < 16) {
      const bool isk = (t == 8) || (t & 1 && t < 15);
      const int blk = t == 8 ? 0 : (t == 15 ? 3 : (isk ? (t - 7) / 2 : (t - 10) / 2));
      const unsigned row = 4u * i + g;
      const unsigned ch = isk ? (li ^ (row & 15u)) : (li ^ ((row & 7u) << 1));
      dma<1>(isk ? rk : rv, row * 256u + ch * 16u, (unsigned)blk * 32768u + pb * 1024u, dst);
    } else {
      const unsigned nb = (((unsigned)(t - 16) >> 1) + nrot) & 3u, kh = (unsigned)(t - 16) & 1u;
      const unsigned row = 4u * i + g;
      const unsigned ch = li ^ (fsw(row) << 1);
      dma<0>(rwo, row * 1024u + ch * 16u, (h0 * 128u + kh * 128u + pb * 4u) * 1024u + nb * 256u, dst);
    }
  };
  auto issue8 = [&](int t) {
#pragma unroll
    for (int i = 0; i < 8; ++i) piece(t, c * 8u, i);
  };

  if (loader) {
#pragma unroll
    for (int i = 0; i < 12; ++i) {
      const unsigned row = c * 12u + i;
      const unsigned ch = lane ^ (row & 15u);
      dma<0>(rx, ch * 16u, row * 1024u, lb + XOFF + row * 1024u);
    }
    unsigned wvo[8];
#pragma unroll
    for (int i = 0; i < 8; ++i) { const unsigned row = 2u * i + (lane >> 5); wvo[i] = row * 8192u + (((lane & 31u) ^ (fsw(row) << 1)) * 16u); }
    auto issuewq = [&](unsigned t, unsigned slot) {
      const unsigned so = ((((t + rot) & 7u) * 64u + c * 16u) * 8192u) + h0 * 256u, dst = lb + ROFF + slot * SLOT + c * 8192u;
#pragma unroll
      for (int i = 0; i < 8; ++i) dma<0>(rwq, wvo[i], so, dst + 1024u * i);
    };
#pragma nounroll
    for (unsigned t = 0; t < 3u; ++t) issuewq(t, t);
    if (tid < 96u) *(LAS float*)(uintptr_t)(lb + SMAXOFF + tid * 4u) = -__builtin_inff();
    gettag();
    waitv<16>();
    BARRIER();
    unsigned slot = 0;
#pragma nounroll
    for (unsigned d = 0; d < 5u; ++d) { waitv<8>(); BARRIER(); issuewq(d + 3u, slot); slot = slot == 2u ? 0u : slot + 1u; }
#define L1STEP(d) do { if ((d) < 7) waitv<8>(); BARRIER(); issue8((d) + 3); } while (0)
    L1STEP(5); L1STEP(6); L1STEP(7);
  } else {
    gettag();
    f32x4 qa[4][3];
#pragma unroll
    for (int t = 0; t < 4; ++t)
#pragma unroll
      for (int j = 0; j < 3; ++j) qa[t][j] = f32x4{0.f, 0.f, 0.f, 0.f};
    bf16x8 fa[2][4], fb[2][3];
    const unsigned wab = lb + ROFF + (8u * g + q4) * 512u + (((8u * c + (p4 >> 1)) ^ (fsw(8u * g + q4) << 1)) * 16u) + 8u * (p4 & 1u);
    const unsigned xab = lb + XOFF + li * 1024u + ((g ^ li) * 16u);
    auto rda = [&](unsigned sb, int ks, int t) {
      const unsigned a = (wab ^ (unsigned)(t << 5)) + sb;
      fa[ks][t] = cat(lds_tr(a + 16384u * ks), lds_tr(a + 16384u * ks + 2048u));
    };
    auto rdb = [&](unsigned kb, int ks) {
      const unsigned a = xab ^ (kb << 7) ^ (unsigned)(ks << 6);
#pragma unroll
      for (int j = 0; j < 3; ++j) fb[ks][j] = lds_b128(a + 16384u * j);
    };
    BARRIER();
#pragma unroll
    for (int ks = 0; ks < 2; ++ks) { rda(0u, ks, 0); rdb(rot, ks); rda(0u, ks, 1); rda(0u, ks, 2); rda(0u, ks, 3); }
    unsigned sb = SLOT;
#pragma nounroll
    for (unsigned d = 0; d < 7u; ++d) {
      BARRIER();
      const unsigned kb = (d + 1u + rot) & 7u;
#pragma unroll
      for (int q = 0; q < 8; ++q) {
#pragma unroll
        for (int j = 0; j < 3; ++j) qa[q & 3][j] = mfma(fa[q >> 2][q & 3], fb[q >> 2][j], qa[q & 3][j]);
        SB();
        rda(sb, q >> 2, q & 3); if ((q & 3) == 3) rdb(kb, q >> 2);
        SB();
      }
      sb = sb == 2u * SLOT ? 0u : sb + SLOT;
    }
    BARRIER();
#pragma unroll
    for (int q = 0; q < 8; ++q)
#pragma unroll
      for (int j = 0; j < 3; ++j) qa[q & 3][j] = mfma(fa[q >> 2][q & 3], fb[q >> 2][j], qa[q & 3][j]);
#pragma unroll
    for (int t = 0; t < 4; ++t)
#pragma unroll
      for (int j = 0; j < 3; ++j) {
        const unsigned row = (c >> 1) * 48u + 16u * j + li;
        const unsigned d = 64u * (c & 1u) + 16u * t + 4u * g;
        u32x2 v = {cvtpk(qa[t][j][0], qa[t][j][1]), cvtpk(qa[t][j][2], qa[t][j][3])};
        *(LAS u32x2*)(uintptr_t)(lb + XOFF + row * 256u + (((d >> 3) ^ (row & 15u)) * 16u) + (d & 7u) * 2u) = v;
      }
  }

  const float CS = 0.088388347648318447f * 1.4426950408889634f;
  LAS float* smax = (LAS float*)(uintptr_t)(lb + SMAXOFF);
  LAS float* ssum = (LAS float*)(uintptr_t)(lb + SSUMOFF);
  bf16x8 qf[3][4], ka[2][4], vf[4][2], pf[4][3], wf[4][2];
  f32x4 sc[2][3], oa[2][3], lacc[3];
  float mrun[3], alpha[3];
#pragma unroll
  for (int j = 0; j < 3; ++j) {
    lacc[j] = f32x4{0.f, 0.f, 0.f, 0.f};
    oa[0][j] = f32x4{0.f, 0.f, 0.f, 0.f};
    oa[1][j] = f32x4{0.f, 0.f, 0.f, 0.f};
    mrun[j] = 0.f; alpha[j] = 1.f;
  }
  const bf16x8 ones = {0x3f80, 0x3f80, 0x3f80, 0x3f80, 0x3f80, 0x3f80, 0x3f80, 0x3f80};
  auto rdk = [&](int t, int kt, int s) {
    ka[kt][s] = lds_b128(lb + ROFF + (unsigned)(t % 3) * SLOT + (32u * c + 16u * kt + li) * 256u + (((4u * s + g) ^ li) * 16u));
  };
  auto rdv = [&](int t, int ks) {
    const unsigned row0 = 4u * g + q4;
    const unsigned a0 = lb + ROFF + (unsigned)(t % 3) * SLOT + row0 * 256u + ((((row0 & 7u) ^ (2u * c)) << 5)) + (p4 << 3);
#pragma unroll
    for (int tt = 0; tt < 2; ++tt) vf[ks][tt] = cat(lds_tr((a0 ^ (unsigned)(tt << 5)) + 8192u * ks), lds_tr((a0 ^ (unsigned)(tt << 5)) + 8192u * ks + 4096u));
  };
  auto rdp = [&](int js, int ks) {
    const unsigned pb = lb + XOFF + (unsigned)(js & 1) * PBUF + r * 12288u + lane * 16u;
#pragma unroll
    for (int j = 0; j < 3; ++j) pf[ks][j] = lds_b128(pb + (unsigned)(ks * 3 + j) * 1024u);
  };
  auto rdw = [&](int e, int ks) {
    const unsigned sb = lb + ROFF + (unsigned)((16 + e) % 3) * SLOT;
#pragma unroll
    for (int t = 0; t < 2; ++t) {
      bf16x4 h[2];
#pragma unroll
      for (int u = 0; u < 2; ++u) {
        const unsigned row = 32u * ks + 8u * g + 4u * u + q4;
        const unsigned ch = (4u * c + 2u * t + (p4 >> 1)) ^ (fsw(row) << 1);
        h[u] = lds_tr(sb + row * 256u + ch * 16u + 8u * (p4 & 1u));
      }
      wf[ks][t] = cat(h[0], h[1]);
    }
  };
  auto pvk = [&](int ks) {
#pragma unroll
    for (int t = 0; t < 2; ++t)
#pragma unroll
      for (int j = 0; j < 3; ++j) oa[t][j] = mfma(vf[ks][t], pf[ks][j], oa[t][j]);
  };
  auto rescale = [&]() {
#pragma unroll
    for (int t = 0; t < 2; ++t)
#pragma unroll
      for (int j = 0; j < 3; ++j) oa[t][j] *= alpha[j];
  };
  auto expo1 = [&](int js, int j) {
    const float nm = -mrun[j] * CS;
    float pe[8];
#pragma unroll
    for (int e = 0; e < 8; ++e) pe[e] = __builtin_amdgcn_exp2f(fmaf(sc[e >> 2][j][e & 3], CS, nm));
    const u32x4 pk = {cvtpk(pe[0], pe[1]), cvtpk(pe[2], pe[3]), cvtpk(pe[4], pe[5]), cvtpk(pe[6], pe[7])};
    if (js > 0) lacc[j][0] *= alpha[j];
    lacc[j] = mfma(ones, __builtin_bit_cast(bf16x8, pk), lacc[j]);
    *(LAS u32x4*)(uintptr_t)(lb + XOFF + (unsigned)(js & 1) * PBUF + (w * 3u + j) * 1024u + lane * 16u) = pk;
  };
#define OPEN(n) do { if ((n) == 0) waitv<16>(); else if ((n) == 1) waitv<8>(); else waitv<4>(); BARRIER(); } while (0)
#define AINT(js) do { \
    OPEN(2 * (js)); \
    if ((js) == 0) { \
      _Pragma("unroll") for (int s = 0; s < 4; ++s) { rdk(8, 0, s); \
        _Pragma("unroll") for (int j = 0; j < 3; ++j) { const unsigned row = r * 48u + 16u * j + li; \
          qf[j][s] = lds_b128(lb + XOFF + row * 256u + (((4u * s + g) ^ (row & 15u)) * 16u)); } } \
      _Pragma("unroll") for (int s = 0; s < 4; ++s) rdk(8, 1, s); \
      SB(); } \
    _Pragma("unroll") for (int kt = 0; kt < 2; ++kt) { \
      _Pragma("unroll") for (int j = 0; j < 3; ++j) sc[kt][j] = f32x4{0.f, 0.f, 0.f, 0.f}; \
      _Pragma("unroll") for (int s = 0; s < 4; ++s) { \
        _Pragma("unroll") for (int j = 0; j < 3; ++j) sc[kt][j] = mfma(ka[kt][s], qf[j][s], sc[kt][j]); \
        SB(); \
        if ((js) >= 1) { if (kt == 0) { rdv(8 + 2 * (js), s); piece(10 + 2 * (js), w * 4u, s); } else rdp((js) - 1, s); } \
        if ((js) >= 2 && s < 3) oa[kt][s] *= alpha[s]; \
        SB(); \
      } } \
    _Pragma("unroll") for (int j = 0; j < 3; ++j) { \
      const float m = max2f(max3f(sc[0][j][0], sc[0][j][1], sc[0][j][2]), max3f(max3f(sc[0][j][3], sc[1][j][0], sc[1][j][1]), sc[1][j][2], sc[1][j][3])); \
      lds_fmax(lb + SMAXOFF + (r * 48u + 16u * j + li) * 4u, m); } \
  } while (0)
#define BINT(js) do { \
    OPEN(2 * (js) + 1); \
    _Pragma("unroll") for (int j = 0; j < 3; ++j) { \
      const float m = smax[r * 48u + 16u * j + li]; \
      if ((js) > 0) alpha[j] = __builtin_amdgcn_exp2f((mrun[j] - m) * CS); \
      mrun[j] = m; } \
    _Pragma("unroll") for (int ks = 0; ks < 4; ++ks) { \
      if ((js) >= 1) pvk(ks); \
      if (ks < 3) expo1(js, ks); \
      if ((js) >= 1 && ks < 3) { _Pragma("unroll") for (int z = 0; z < 6; ++z) { __builtin_amdgcn_sched_group_barrier(0x8, 1, 0); __builtin_amdgcn_sched_group_barrier(0x402, 4, 0); } } \
      SB(); \
      if ((js) < 3) { rdk(9 + 2 * (js), 0, ks); rdk(9 + 2 * (js), 1, ks); } else rdv(15, ks); \
      piece(11 + 2 * (js), w * 4u, ks); \
      SB(); \
    } \
  } while (0)
  AINT(0); BINT(0); AINT(1); BINT(1); AINT(2); BINT(2); AINT(3); BINT(3);
#pragma unroll
  for (int j = 0; j < 3; ++j) { ssum[(r * 4u + c) * 48u + 16u * j + li] = lacc[j][0]; }
  OPEN(8);
#pragma unroll
  for (int ks = 0; ks < 4; ++ks) rdp(3, ks);
  rescale();
  float inv[3];
#pragma unroll
  for (int j = 0; j < 3; ++j)
    inv[j] = __builtin_amdgcn_rcpf((ssum[(r * 4u + 0u) * 48u + 16u * j + li] + ssum[(r * 4u + 1u) * 48u + 16u * j + li]) +
                                   (ssum[(r * 4u + 2u) * 48u + 16u * j + li] + ssum[(r * 4u + 3u) * 48u + 16u * j + li]));
#pragma unroll
  for (int ks = 0; ks < 4; ++ks) {
    pvk(ks);
    SB();
    piece(18, w * 4u, ks);
    if (!loader) rdw(0, ks);
    SB();
  }

#pragma unroll
  for (int j = 0; j < 3; ++j)
#pragma unroll
    for (int t = 0; t < 2; ++t) {
      const unsigned row = 16u * j + li;
      const unsigned k = r * 128u + 32u * c + 16u * t + 4u * g;
      u32x2 v = {cvtpk(oa[t][j][0] * inv[j], oa[t][j][1] * inv[j]), cvtpk(oa[t][j][2] * inv[j], oa[t][j][3] * inv[j])};
      *(LAS u32x2*)(uintptr_t)(lb + XOFF + row * 512u + (((k >> 3) ^ li) * 16u) + (k & 7u) * 2u) = v;
    }

  if (loader) {
#define L3STEP(e) do { if ((e) == 0) waitv<4>(); else if ((e) < 6) waitv<8>(); else waitv<0>(); BARRIER(); if ((e) + 3 < 8) issue8(19 + (e)); } while (0)
    L3STEP(0); L3STEP(1); L3STEP(2); L3STEP(3); L3STEP(4); L3STEP(5); L3STEP(6);
    BARRIER();
    if (c == 0) {
      for (unsigned spin = 0; spin < (1u << 17); ++spin) {
        const unsigned long long f = __hip_atomic_load(flags + hp * 16u + li, __ATOMIC_RELAXED, __HIP_MEMORY_SCOPE_AGENT);
        if (__all(f == tag)) break;
        __builtin_amdgcn_s_sleep(POLLSLEEP);
      }
    }
    BARRIER();
    if (c < 3) {

      u32x4 v[16];
#pragma unroll
      for (int p = 0; p < 16; ++p) v[p] = __builtin_amdgcn_raw_buffer_load_b128(rp, ((hp * 3u + c) * 64u + lane) * 16u, p * (SQ * DM * 2), 16);
      float sum[8];
#pragma unroll
      for (int k = 0; k < 8; ++k) sum[k] = 0.f;
#pragma unroll
      for (int p = 0; p < 16; ++p)
#pragma unroll
        for (int k = 0; k < 4; ++k) {
          sum[2 * k] += __uint_as_float(v[p][k] << 16);
          sum[2 * k + 1] += __uint_as_float(v[p][k] & 0xffff0000u);
        }
      unsigned short* o = out + ((size_t)b * SQ + 16u * c + li) * DM + 32u * hp + 4u * g;
      *(u32x2*)o = u32x2{cvtpk(sum[0], sum[1]), cvtpk(sum[2], sum[3])};
      *(u32x2*)(o + 16) = u32x2{cvtpk(sum[4], sum[5]), cvtpk(sum[6], sum[7])};
      if (hp == 0 && c == 0 && lane == 0) __hip_atomic_store(epoch, tag, __ATOMIC_RELAXED, __HIP_MEMORY_SCOPE_AGENT);
    }
  } else {
    bf16x8 of[3][8];
    f32x4 pa[2][3];
#define C3STEP(e) do { \
      if ((e) < 7) { if ((e) == 0) waitv<4>(); else if ((e) == 1) waitv<0>(); BARRIER(); } \
      if ((e) == 0) { \
        _Pragma("unroll") for (int s = 0; s < 8; ++s) \
        _Pragma("unroll") for (int j = 0; j < 3; ++j) of[j][s] = lds_b128(lb + XOFF + (16u * j + li) * 512u + (((4u * s + g) ^ li) * 16u)); } \
      if (!((e) & 1)) { _Pragma("unroll") for (int j = 0; j < 3; ++j) { pa[0][j] = f32x4{0.f, 0.f, 0.f, 0.f}; pa[1][j] = f32x4{0.f, 0.f, 0.f, 0.f}; } } \
      SB(); \
      _Pragma("unroll") for (int ks = 0; ks < 4; ++ks) { \
        _Pragma("unroll") for (int t = 0; t < 2; ++t) \
        _Pragma("unroll") for (int j = 0; j < 3; ++j) pa[t][j] = mfma(wf[ks][t], of[j][4 * ((e) & 1) + ks], pa[t][j]); \
        SB(); if ((e) + 1 < 8) rdw((e) + 1, ks); SB(); \
      } \
      if ((e) & 1) { const unsigned nb = (((unsigned)(e) >> 1) + nrot) & 3u; \
        _Pragma("unroll") for (int j = 0; j < 3; ++j) \
          __builtin_amdgcn_raw_buffer_store_b128(u32x4{cvtpk(pa[0][j][0], pa[0][j][1]), cvtpk(pa[0][j][2], pa[0][j][3]), cvtpk(pa[1][j][0], pa[1][j][1]), cvtpk(pa[1][j][2], pa[1][j][3])}, \
                                                 rp, lane * 16u + 1024u * j, hp * (SQ * DM * 2) + (nb * 4u + c) * 3072u, 16); \
        if ((e) == 7) { waitv<0>(); \
          if (lane < 4u) __hip_atomic_store(flags + (lane * 4u + c) * 16u + hp, tag, __ATOMIC_RELAXED, __HIP_MEMORY_SCOPE_AGENT); } } \
    } while (0)
    C3STEP(0); C3STEP(1); C3STEP(2); C3STEP(3); C3STEP(4); C3STEP(5); C3STEP(6); C3STEP(7);
    BARRIER(); BARRIER();
  }
}

extern "C" void kernel_launch(void* const* d_in, const int* in_sizes, int n_in,
                              void* d_out, int out_size, void* d_ws, size_t ws_size,
                              hipStream_t stream) {
  (void)in_sizes; (void)n_in; (void)out_size; (void)ws_size;
  (void)hipFuncSetAttribute((const void*)gqa_fused, hipFuncAttributeMaxDynamicSharedMemorySize, LDS_BYTES);
  const unsigned short* x = (const unsigned short*)d_in[0];
  const unsigned short* kc = (const unsigned short*)d_in[1];
  const unsigned short* vc = (const unsigned short*)d_in[2];
  const unsigned short* wq = (const unsigned short*)d_in[3];
  const unsigned short* wo = (const unsigned short*)d_in[4];
  unsigned short* part = (unsigned short*)d_ws;
  hipLaunchKernelGGL(gqa_fused, dim3(256), dim3(512), LDS_BYTES, stream, x, kc, vc, wq, wo, part, (unsigned short*)d_out);
}
```

```cpp
#include <hip/hip_runtime.h>
#include <hip/hip_bf16.h>
#include <stdint.h>

typedef short bf16x8 __attribute__((ext_vector_type(8)));
typedef short bf16x4 __attribute__((ext_vector_type(4)));
typedef float f32x4 __attribute__((ext_vector_type(4)));
typedef float f32x2 __attribute__((ext_vector_type(2)));
typedef __bf16 bf2 __attribute__((ext_vector_type(2)));
typedef unsigned u32x2 __attribute__((ext_vector_type(2)));
typedef unsigned u32x4 __attribute__((ext_vector_type(4)));
#define LAS __attribute__((address_space(3)))
#define DI __device__ __forceinline__

constexpr int NB = 16, SQ = 48, DM = 512, HQ = 32, HKV = 8, SK = 512, DH = 128;
constexpr unsigned XOFF = 0;
constexpr unsigned ROFF = 65536;
constexpr unsigned SLOT = 32768;
constexpr unsigned PBUF = 24576;
constexpr unsigned SMAXOFF = 49152, SSUMOFF = 51200;
constexpr unsigned LDS_BYTES = 163840;
#define POLLSLEEP 4
constexpr size_t FLAG_OFF = 16u << 20, EPOCH_OFF = 17u << 20;
typedef __attribute__((address_space(1))) unsigned long long gu64;

DI unsigned rfl(unsigned x) { return __builtin_amdgcn_readfirstlane(x); }
DI unsigned cvtpk(float lo, float hi) {
  f32x2 v = {lo, hi};
  bf2 b = __builtin_convertvector(v, bf2);
  return __builtin_bit_cast(unsigned, b);
}
#define BARRIER() asm volatile("s_waitcnt lgkmcnt(0)\n\ts_barrier" ::: "memory")
#define SB() __builtin_amdgcn_sched_barrier(0)
template <int N> DI void waitv() { asm volatile("s_waitcnt vmcnt(%0)" :: "i"(N > 63 ? 63 : N) : "memory"); }
template <int NT> DI void dma(__amdgpu_buffer_rsrc_t rsrc, unsigned voff, unsigned soff, unsigned ldsdst) {
  if (NT) asm volatile("s_mov_b32 m0, %0\n\ts_nop 0\n\tbuffer_load_dwordx4 %1, %2, %3 offen nt lds"
               :: "s"(ldsdst), "v"(voff), "s"(rsrc), "s"(soff) : "memory");
  else asm volatile("s_mov_b32 m0, %0\n\ts_nop 0\n\tbuffer_load_dwordx4 %1, %2, %3 offen lds"
               :: "s"(ldsdst), "v"(voff), "s"(rsrc), "s"(soff) : "memory");
}
DI bf16x8 lds_b128(unsigned a) { return *(const LAS bf16x8*)(uintptr_t)a; }
DI bf16x4 lds_tr(unsigned a) { return __builtin_amdgcn_ds_read_tr16_b64_v4i16((LAS bf16x4*)(uintptr_t)a); }
DI bf16x8 cat(bf16x4 a, bf16x4 b) { return __builtin_shufflevector(a, b, 0, 1, 2, 3, 4, 5, 6, 7); }
DI f32x4 mfma(bf16x8 a, bf16x8 b, f32x4 c) { return __builtin_amdgcn_mfma_f32_16x16x32_bf16(a, b, c, 0, 0, 0); }
DI unsigned fsw(unsigned row) { return (row & 3u) | (((row >> 3) & 1u) << 2); }
DI float max2f(float a, float b) { return __builtin_elementwise_maximum(a, b); }
DI float max3f(float a, float b, float c) { return max2f(max2f(a, b), c); }
DI void lds_fmax(unsigned a, float v) { asm volatile("ds_max_f32 %0, %1" :: "v"(a), "v"(v) : "memory"); }

__global__ __launch_bounds__(512, 2)
void gqa_fused(const unsigned short* __restrict__ x, const unsigned short* __restrict__ kc,
               const unsigned short* __restrict__ vc, const unsigned short* __restrict__ wq,
               const unsigned short* __restrict__ wo, unsigned short* __restrict__ part,
               unsigned short* __restrict__ out) {
  extern __shared__ __attribute__((aligned(16))) char smem[];
  const unsigned lb = (unsigned)(uintptr_t)(LAS char*)smem;
  const unsigned tid = threadIdx.x, lane = tid & 63u;
  const unsigned w = rfl(tid >> 6);
  const unsigned bid = blockIdx.x, hkv = bid & 7u, idx = bid >> 3, b = idx >> 1, half = idx & 1u;
  const unsigned h0 = hkv * 4u + half * 2u;
  const unsigned g = lane >> 4, li = lane & 15u, q4 = li >> 2, p4 = lane & 3u;
  const unsigned r = w >> 2, c = w & 3u;
  const unsigned rot = b & 7u, nrot = b & 3u;
  const bool loader = (r == 0);

  const __amdgpu_buffer_rsrc_t rx = __builtin_amdgcn_make_buffer_rsrc((void*)(x + (size_t)b * SQ * DM), (short)0, SQ * DM * 2, 0x00020000);
  const __amdgpu_buffer_rsrc_t rwq = __builtin_amdgcn_make_buffer_rsrc((void*)wq, (short)0, DM * HQ * DH * 2, 0x00020000);
  const __amdgpu_buffer_rsrc_t rwo = __builtin_amdgcn_make_buffer_rsrc((void*)wo, (short)0, DM * HQ * DH * 2, 0x00020000);
  const __amdgpu_buffer_rsrc_t rk = __builtin_amdgcn_make_buffer_rsrc((void*)(kc + ((size_t)b * HKV + hkv) * SK * DH), (short)0, SK * DH * 2, 0x00020000);
  const __amdgpu_buffer_rsrc_t rv = __builtin_amdgcn_make_buffer_rsrc((void*)(vc + ((size_t)b * HKV + hkv) * SK * DH), (short)0, SK * DH * 2, 0x00020000);
  const unsigned hp = hkv * 2u + half;
  const __amdgpu_buffer_rsrc_t rp = __builtin_amdgcn_make_buffer_rsrc((void*)(part + (size_t)b * 16u * (SQ * DM)), (short)0, 16 * SQ * DM * 2, 0x00020000);
  gu64* flags = (gu64*)((char*)part + FLAG_OFF) + (size_t)b * 256u;
  gu64* epoch = (gu64*)((char*)part + EPOCH_OFF) + (size_t)b * 16u;
  unsigned long long tag;
  auto gettag = [&]() {
    unsigned long long e;
    asm volatile("s_load_dwordx2 %0, %1, 0x0\n\ts_waitcnt lgkmcnt(0)" : "=&s"(e) : "s"(epoch) : "memory");
    tag = e * 0x9E3779B97F4A7C15ull + 0xD1B54A32D192ED03ull;
  };

  auto piece = [&](int t, unsigned pb, int i) {
    const unsigned dst = lb + ROFF + (unsigned)(t % 3) * SLOT + pb * 1024u + 1024u * i;
    if (t < 8) {
      const unsigned kb = ((unsigned)t + rot) & 7u;
      const unsigned row = 2u * i + (lane >> 5);
      const unsigned ch = (lane & 31u) ^ (fsw(row) << 1);
      dma<0>(rwq, row * 8192u + ch * 16u, (kb * 64u + pb * 2u) * 8192u + h0 * 256u, dst);
    } else if (t < 16) {
      const bool isk = (t == 8) || (t & 1 && t < 15);
      const int blk = t == 8 ? 0 : (t == 15 ? 3 : (isk ? (t - 7) / 2 : (t - 10) / 2));
      const unsigned row = 4u * i + g;
      const unsigned ch = isk ? (li ^ (row & 15u)) : (li ^ ((row & 7u) << 1));
      dma<1>(isk ? rk : rv, row * 256u + ch * 16u, (unsigned)blk * 32768u + pb * 1024u, dst);
    } else {
      const unsigned nb = (((unsigned)(t - 16) >> 1) + nrot) & 3u, kh = (unsigned)(t - 16) & 1u;
      const unsigned row = 4u * i + g;
      const unsigned ch = li ^ (fsw(row) << 1);
      dma<0>(rwo, row * 1024u + ch * 16u, (h0 * 128u + kh * 128u + pb * 4u) * 1024u + nb * 256u, dst);
    }
  };
  auto issue8 = [&](int t) {
#pragma unroll
    for (int i = 0; i < 8; ++i) piece(t, c * 8u, i);
  };

  if (loader) {
#pragma unroll
    for (int i = 0; i < 12; ++i) {
      const unsigned row = c * 12u + i;
      const unsigned ch = lane ^ (row & 15u);
      dma<0>(rx, ch * 16u, row * 1024u, lb + XOFF + row * 1024u);
    }
    unsigned wvo[8];
#pragma unroll
    for (int i = 0; i < 8; ++i) { const unsigned row = 2u * i + (lane >> 5); wvo[i] = row * 8192u + (((lane & 31u) ^ (fsw(row) << 1)) * 16u); }
    auto issuewq = [&](unsigned t, unsigned slot) {
      const unsigned so = ((((t + rot) & 7u) * 64u + c * 16u) * 8192u) + h0 * 256u, dst = lb + ROFF + slot * SLOT + c * 8192u;
#pragma unroll
      for (int i = 0; i < 8; ++i) dma<0>(rwq, wvo[i], so, dst + 1024u * i);
    };
#pragma nounroll
    for (unsigned t = 0; t < 3u; ++t) issuewq(t, t);
    if (tid < 96u) *(LAS float*)(uintptr_t)(lb + SMAXOFF + tid * 4u) = -__builtin_inff();
    gettag();
    waitv<16>();
    BARRIER();
    unsigned slot = 0;
#pragma nounroll
    for (unsigned d = 0; d < 5u; ++d) { waitv<8>(); BARRIER(); issuewq(d + 3u, slot); slot = slot == 2u ? 0u : slot + 1u; }
#define L1STEP(d) do { if ((d) < 7) waitv<8>(); BARRIER(); issue8((d) + 3); } while (0)
    L1STEP(5); L1STEP(6); L1STEP(7);
  } else {
    gettag();
    f32x4 qa[4][3];
#pragma unroll
    for (int t = 0; t < 4; ++t)
#pragma unroll
      for (int j = 0; j < 3; ++j) qa[t][j] = f32x4{0.f, 0.f, 0.f, 0.f};
    bf16x8 fa[2][4], fb[2][3];
    const unsigned wab = lb + ROFF + (8u * g + q4) * 512u + (((8u * c + (p4 >> 1)) ^ (fsw(8u * g + q4) << 1)) * 16u) + 8u * (p4 & 1u);
    const unsigned xab = lb + XOFF + li * 1024u + ((g ^ li) * 16u);
    auto rda = [&](unsigned sb, int ks, int t) {
      const unsigned a = (wab ^ (unsigned)(t << 5)) + sb;
      fa[ks][t] = cat(lds_tr(a + 16384u * ks), lds_tr(a + 16384u * ks + 2048u));
    };
    auto rdb = [&](unsigned kb, int ks) {
      const unsigned a = xab ^ (kb << 7) ^ (unsigned)(ks << 6);
#pragma unroll
      for (int j = 0; j < 3; ++j) fb[ks][j] = lds_b128(a + 16384u * j);
    };
    BARRIER();
#pragma unroll
    for (int ks = 0; ks < 2; ++ks) { rda(0u, ks, 0); rdb(rot, ks); rda(0u, ks, 1); rda(0u, ks, 2); rda(0u, ks, 3); }
    unsigned sb = SLOT;
#pragma nounroll
    for (unsigned d = 0; d < 7u; ++d) {
      BARRIER();
      const unsigned kb = (d + 1u + rot) & 7u;
#pragma unroll
      for (int q = 0; q < 8; ++q) {
#pragma unroll
        for (int j = 0; j < 3; ++j) qa[q & 3][j] = mfma(fa[q >> 2][q & 3], fb[q >> 2][j], qa[q & 3][j]);
        SB();
        rda(sb, q >> 2, q & 3); if ((q & 3) == 3) rdb(kb, q >> 2);
        SB();
      }
      sb = sb == 2u * SLOT ? 0u : sb + SLOT;
    }
    BARRIER();
#pragma unroll
    for (int q = 0; q < 8; ++q)
#pragma unroll
      for (int j = 0; j < 3; ++j) qa[q & 3][j] = mfma(fa[q >> 2][q & 3], fb[q >> 2][j], qa[q & 3][j]);
#pragma unroll
    for (int t = 0; t < 4; ++t)
#pragma unroll
      for (int j = 0; j < 3; ++j) {
        const unsigned row = (c >> 1) * 48u + 16u * j + li;
        const unsigned d = 64u * (c & 1u) + 16u * t + 4u * g;
        u32x2 v = {cvtpk(qa[t][j][0], qa[t][j][1]), cvtpk(qa[t][j][2], qa[t][j][3])};
        *(LAS u32x2*)(uintptr_t)(lb + XOFF + row * 256u + (((d >> 3) ^ (row & 15u)) * 16u) + (d & 7u) * 2u) = v;
      }
  }

  const float CS = 0.088388347648318447f * 1.4426950408889634f;
  LAS float* smax = (LAS float*)(uintptr_t)(lb + SMAXOFF);
  LAS float* ssum = (LAS float*)(uintptr_t)(lb + SSUMOFF);
  bf16x8 qf[3][4], ka[2][4], vf[4][2], pf[4][3], wf[4][2];
  f32x4 sc[2][3], oa[2][3], lacc[3];
  float mrun[3], alpha[3];
#pragma unroll
  for (int j = 0; j < 3; ++j) {
    lacc[j] = f32x4{0.f, 0.f, 0.f, 0.f};
    oa[0][j] = f32x4{0.f, 0.f, 0.f, 0.f};
    oa[1][j] = f32x4{0.f, 0.f, 0.f, 0.f};
    mrun[j] = 0.f; alpha[j] = 1.f;
  }
  const bf16x8 ones = {0x3f80, 0x3f80, 0x3f80, 0x3f80, 0x3f80, 0x3f80, 0x3f80, 0x3f80};
  auto rdk = [&](int t, int kt, int s) {
    ka[kt][s] = lds_b128(lb + ROFF + (unsigned)(t % 3) * SLOT + (32u * c + 16u * kt + li) * 256u + (((4u * s + g) ^ li) * 16u));
  };
  auto rdv = [&](int t, int ks) {
    const unsigned row0 = 4u * g + q4;
    const unsigned a0 = lb + ROFF + (unsigned)(t % 3) * SLOT + row0 * 256u + ((((row0 & 7u) ^ (2u * c)) << 5)) + (p4 << 3);
#pragma unroll
    for (int tt = 0; tt < 2; ++tt) vf[ks][tt] = cat(lds_tr((a0 ^ (unsigned)(tt << 5)) + 8192u * ks), lds_tr((a0 ^ (unsigned)(tt << 5)) + 8192u * ks + 4096u));
  };
  auto rdp = [&](int js, int ks) {
    const unsigned pb = lb + XOFF + (unsigned)(js & 1) * PBUF + r * 12288u + lane * 16u;
#pragma unroll
    for (int j = 0; j < 3; ++j) pf[ks][j] = lds_b128(pb + (unsigned)(ks * 3 + j) * 1024u);
  };
  auto rdw = [&](int e, int ks) {
    const unsigned sb = lb + ROFF + (unsigned)((16 + e) % 3) * SLOT;
#pragma unroll
    for (int t = 0; t < 2; ++t) {
      bf16x4 h[2];
#pragma unroll
      for (int u = 0; u < 2; ++u) {
        const unsigned row = 32u * ks + 8u * g + 4u * u + q4;
        const unsigned ch = (4u * c + 2u * t + (p4 >> 1)) ^ (fsw(row) << 1);
        h[u] = lds_tr(sb + row * 256u + ch * 16u + 8u * (p4 & 1u));
      }
      wf[ks][t] = cat(h[0], h[1]);
    }
  };
  auto pvk = [&](int ks) {
#pragma unroll
    for (int t = 0; t < 2; ++t)
#pragma unroll
      for (int j = 0; j < 3; ++j) oa[t][j] = mfma(vf[ks][t], pf[ks][j], oa[t][j]);
  };
  auto rescale = [&]() {
#pragma unroll
    for (int t = 0; t < 2; ++t)
#pragma unroll
      for (int j = 0; j < 3; ++j) oa[t][j] *= alpha[j];
  };
  auto expo1 = [&](int js, int j) {
    const float nm = -mrun[j] * CS;
    float pe[8];
#pragma unroll
    for (int e = 0; e < 8; ++e) pe[e] = __builtin_amdgcn_exp2f(fmaf(sc[e >> 2][j][e & 3], CS, nm));
    const u32x4 pk = {cvtpk(pe[0], pe[1]), cvtpk(pe[2], pe[3]), cvtpk(pe[4], pe[5]), cvtpk(pe[6], pe[7])};
    if (js > 0) lacc[j][0] *= alpha[j];
    lacc[j] = mfma(ones, __builtin_bit_cast(bf16x8, pk), lacc[j]);
    *(LAS u32x4*)(uintptr_t)(lb + XOFF + (unsigned)(js & 1) * PBUF + (w * 3u + j) * 1024u + lane * 16u) = pk;
  };
#define OPEN(n) do { if ((n) == 0) waitv<16>(); else if ((n) == 1) waitv<8>(); else waitv<4>(); BARRIER(); } while (0)
#define AINT(js) do { \
    OPEN(2 * (js)); \
    if ((js) == 0) { \
      _Pragma("unroll") for (int s = 0; s < 4; ++s) { rdk(8, 0, s); \
        _Pragma("unroll") for (int j = 0; j < 3; ++j) { const unsigned row = r * 48u + 16u * j + li; \
          qf[j][s] = lds_b128(lb + XOFF + row * 256u + (((4u * s + g) ^ (row & 15u)) * 16u)); } } \
      _Pragma("unroll") for (int s = 0; s < 4; ++s) rdk(8, 1, s); \
      SB(); } \
    _Pragma("unroll") for (int kt = 0; kt < 2; ++kt) { \
      _Pragma("unroll") for (int j = 0; j < 3; ++j) sc[kt][j] = f32x4{0.f, 0.f, 0.f, 0.f}; \
      _Pragma("unroll") for (int s = 0; s < 4; ++s) { \
        _Pragma("unroll") for (int j = 0; j < 3; ++j) sc[kt][j] = mfma(ka[kt][s], qf[j][s], sc[kt][j]); \
        SB(); \
        if ((js) >= 1) { if (kt == 0) { rdv(8 + 2 * (js), s); piece(10 + 2 * (js), w * 4u, s); } else rdp((js) - 1, s); } \
        if ((js) >= 2 && s < 3) oa[kt][s] *= alpha[s]; \
        SB(); \
      } } \
    _Pragma("unroll") for (int j = 0; j < 3; ++j) { \
      const float m = max2f(max3f(sc[0][j][0], sc[0][j][1], sc[0][j][2]), max3f(max3f(sc[0][j][3], sc[1][j][0], sc[1][j][1]), sc[1][j][2], sc[1][j][3])); \
      lds_fmax(lb + SMAXOFF + (r * 48u + 16u * j + li) * 4u, m); } \
  } while (0)
#define BINT(js) do { \
    OPEN(2 * (js) + 1); \
    _Pragma("unroll") for (int j = 0; j < 3; ++j) { \
      const float m = smax[r * 48u + 16u * j + li]; \
      if ((js) > 0) alpha[j] = __builtin_amdgcn_exp2f((mrun[j] - m) * CS); \
      mrun[j] = m; } \
    _Pragma("unroll") for (int ks = 0; ks < 4; ++ks) { \
      if ((js) >= 1) pvk(ks); \
      if (ks < 3) expo1(js, ks); \
      if ((js) >= 1 && ks < 3) { _Pragma("unroll") for (int z = 0; z < 6; ++z) { __builtin_amdgcn_sched_group_barrier(0x8, 1, 0); __builtin_amdgcn_sched_group_barrier(0x402, 4, 0); } } \
      SB(); \
      if ((js) < 3) { rdk(9 + 2 * (js), 0, ks); rdk(9 + 2 * (js), 1, ks); } else rdv(15, ks); \
      piece(11 + 2 * (js), w * 4u, ks); \
      SB(); \
    } \
  } while (0)
  AINT(0); BINT(0); AINT(1); BINT(1); AINT(2); BINT(2); AINT(3); BINT(3);
#pragma unroll
  for (int j = 0; j < 3; ++j) { ssum[(r * 4u + c) * 48u + 16u * j + li] = lacc[j][0]; }
  OPEN(8);
#pragma unroll
  for (int ks = 0; ks < 4; ++ks) rdp(3, ks);
  rescale();
  float inv[3];
#pragma unroll
  for (int j = 0; j < 3; ++j)
    inv[j] = __builtin_amdgcn_rcpf((ssum[(r * 4u + 0u) * 48u + 16u * j + li] + ssum[(r * 4u + 1u) * 48u + 16u * j + li]) +
                                   (ssum[(r * 4u + 2u) * 48u + 16u * j + li] + ssum[(r * 4u + 3u) * 48u + 16u * j + li]));
#pragma unroll
  for (int ks = 0; ks < 4; ++ks) {
    pvk(ks);
    SB();
    piece(18, w * 4u, ks);
    if (!loader) rdw(0, ks);
    SB();
  }

#pragma unroll
  for (int j = 0; j < 3; ++j)
#pragma unroll
    for (int t = 0; t < 2; ++t) {
      const unsigned row = 16u * j + li;
      const unsigned k = r * 128u + 32u * c + 16u * t + 4u * g;
      u32x2 v = {cvtpk(oa[t][j][0] * inv[j], oa[t][j][1] * inv[j]), cvtpk(oa[t][j][2] * inv[j], oa[t][j][3] * inv[j])};
      *(LAS u32x2*)(uintptr_t)(lb + XOFF + row * 512u + (((k >> 3) ^ li) * 16u) + (k & 7u) * 2u) = v;
    }

  if (loader) {
#define L3STEP(e) do { if ((e) == 0) waitv<4>(); else if ((e) < 6) waitv<8>(); else waitv<0>(); BARRIER(); if ((e) + 3 < 8) issue8(19 + (e)); } while (0)
    L3STEP(0); L3STEP(1); L3STEP(2); L3STEP(3); L3STEP(4); L3STEP(5); L3STEP(6);
    BARRIER();
    if (c == 0) {
      for (unsigned spin = 0; spin < (1u << 17); ++spin) {
        const unsigned long long f = __hip_atomic_load(flags + hp * 16u + li, __ATOMIC_RELAXED, __HIP_MEMORY_SCOPE_AGENT);
        if (__all(f == tag)) break;
        __builtin_amdgcn_s_sleep(POLLSLEEP);
      }
    }
    BARRIER();
    if (c < 3) {

      u32x4 v[16];
#pragma unroll
      for (int p = 0; p < 16; ++p) v[p] = __builtin_amdgcn_raw_buffer_load_b128(rp, ((hp * 3u + c) * 64u + lane) * 16u, p * (SQ * DM * 2), 16);
      float sum[8];
#pragma unroll
      for (int k = 0; k < 8; ++k) sum[k] = 0.f;
#pragma unroll
      for (int p = 0; p < 16; ++p)
#pragma unroll
        for (int k = 0; k < 4; ++k) {
          sum[2 * k] += __uint_as_float(v[p][k] << 16);
          sum[2 * k + 1] += __uint_as_float(v[p][k] & 0xffff0000u);
        }
      unsigned short* o = out + ((size_t)b * SQ + 16u * c + li) * DM + 32u * hp + 4u * g;
      *(u32x2*)o = u32x2{cvtpk(sum[0], sum[1]), cvtpk(sum[2], sum[3])};
      *(u32x2*)(o + 16) = u32x2{cvtpk(sum[4], sum[5]), cvtpk(sum[6], sum[7])};
      if (hp == 0 && c == 0 && lane == 0) __hip_atomic_store(epoch, tag, __ATOMIC_RELAXED, __HIP_MEMORY_SCOPE_AGENT);
    }
  } else {
    bf16x8 of[3][8];
    f32x4 pa[2][3];
#define C3STEP(e) do { \
      if ((e) < 7) { if ((e) == 0) waitv<4>(); else if ((e) == 1) waitv<0>(); BARRIER(); } \
      if ((e) == 0) { \
        _Pragma("unroll") for (int s = 0; s < 8; ++s) \
        _Pragma("unroll") for (int j = 0; j < 3; ++j) of[j][s] = lds_b128(lb + XOFF + (16u * j + li) * 512u + (((4u * s + g) ^ li) * 16u)); } \
      if (!((e) & 1)) { _Pragma("unroll") for (int j = 0; j < 3; ++j) { pa[0][j] = f32x4{0.f, 0.f, 0.f, 0.f}; pa[1][j] = f32x4{0.f, 0.f, 0.f, 0.f}; } } \
      SB(); \
      _Pragma("unroll") for (int ks = 0; ks < 4; ++ks) { \
        _Pragma("unroll") for (int t = 0; t < 2; ++t) \
        _Pragma("unroll") for (int j = 0; j < 3; ++j) pa[t][j] = mfma(wf[ks][t], of[j][4 * ((e) & 1) + ks], pa[t][j]); \
        SB(); if ((e) + 1 < 8) rdw((e) + 1, ks); SB(); \
      } \
      if ((e) & 1) { const unsigned nb = (((unsigned)(e) >> 1) + nrot) & 3u; \
        _Pragma("unroll") for (int j = 0; j < 3; ++j) \
          __builtin_amdgcn_raw_buffer_store_b128(u32x4{cvtpk(pa[0][j][0], pa[0][j][1]), cvtpk(pa[0][j][2], pa[0][j][3]), cvtpk(pa[1][j][0], pa[1][j][1]), cvtpk(pa[1][j][2], pa[1][j][3])}, \
                                                 rp, lane * 16u + 1024u * j, hp * (SQ * DM * 2) + (nb * 4u + c) * 3072u, 16); \
        if ((e) == 7) { waitv<0>(); \
          if (lane < 4u) __hip_atomic_store(flags + (lane * 4u + c) * 16u + hp, tag, __ATOMIC_RELAXED, __HIP_MEMORY_SCOPE_AGENT); } } \
    } while (0)
    C3STEP(0); C3STEP(1); C3STEP(2); C3STEP(3); C3STEP(4); C3STEP(5); C3STEP(6); C3STEP(7);
    BARRIER(); BARRIER();
  }
}

extern "C" void kernel_launch(void* const* d_in, const int* in_sizes, int n_in,
                              void* d_out, int out_size, void* d_ws, size_t ws_size,
                              hipStream_t stream) {
  (void)in_sizes; (void)n_in; (void)out_size; (void)ws_size;
  (void)hipFuncSetAttribute((const void*)gqa_fused, hipFuncAttributeMaxDynamicSharedMemorySize, LDS_BYTES);
  const unsigned short* x = (const unsigned short*)d_in[0];
  const unsigned short* kc = (const unsigned short*)d_in[1];
  const unsigned short* vc = (const unsigned short*)d_in[2];
  const unsigned short* wq = (const unsigned short*)d_in[3];
  const unsigned short* wo = (const unsigned short*)d_in[4];
  unsigned short* part = (unsigned short*)d_ws;
  hipLaunchKernelGGL(gqa_fused, dim3(256), dim3(512), LDS_BYTES, stream, x, kc, vc, wq, wo, part, (unsigned short*)d_out);
}
```
